```python
import torch

def _softmax(x, axis=-1):
    return torch.softmax(x, dim=axis)
def _gelu(x):
    return torch.nn.functional.gelu(x, approximate="tanh")
def _silu(x):
    return torch.nn.functional.silu(x)
def _sigmoid(x):
    return torch.sigmoid(x)
def _layernorm(x, axis=-1, eps=1e-5):
    m = x.mean(dim=axis, keepdim=True)
    v = x.var(dim=axis, keepdim=True, unbiased=False)
    return (x - m) / torch.sqrt(v + eps)
def _scatter_add(tgt, idx, src, axis=0):
    return tgt.clone().index_add_(axis, idx, src)
def _segment_sum(src, seg, n):
    out = torch.zeros((n,) + src.shape[1:], dtype=src.dtype)
    return out.index_add_(0, seg, src)
def _pad(x, pw):
    flat = tuple(v for pair in reversed(pw) for v in pair)
    return torch.nn.functional.pad(x, flat)

kernel_name = 'concat_heads__sliding_block__sliding_block__prompt'

def setup_inputs():

    _w = (lambda a: (lambda t: t.to(torch.bfloat16) if t.is_floating_point() else t)(torch.as_tensor(a)))
    def _A_setup():
        B, S, D = 1, 2048, 448
        return {
            "x": _w((lambda *s: torch.randn(*s, dtype=torch.bfloat16) * 0.4)(B, S, D)),
            "wqkv": _w((lambda *s: torch.randn(*s, dtype=torch.bfloat16) * 0.4)(D, 3 * D)),
            "wo": _w((lambda *s: torch.randn(*s, dtype=torch.bfloat16) * 0.05)(D, D)),
        }
    _A_in = _A_setup()

    def _B_setup():
        B, S, D = 1, 2048, 448
        return {
            "x": _w((lambda *s: torch.randn(*s, dtype=torch.bfloat16) * 0.4)(B, S, D)),
            "wqkv": _w((lambda *s: torch.randn(*s, dtype=torch.bfloat16) * 0.4)(D, 3 * D)),
            "wo": _w((lambda *s: torch.randn(*s, dtype=torch.bfloat16) * 0.05)(D, D)),
        }
    _B_in = _B_setup()

    return {
        "A_x": _A_in["x"],
        "A_wqkv": _A_in["wqkv"],
        "A_wo": _A_in["wo"],
        "B_x": _B_in["x"],
        "B_wqkv": _B_in["wqkv"],
        "B_wo": _B_in["wo"],
    }

def reference(A_x, A_wqkv, A_wo, B_x, B_wqkv, B_wo):
    def _A_ref(x, wqkv, wo):
        B, S, D = x.shape
        H = 16; Dh = D // H; W = 256
        qkv = (lambda x, *d: x.permute(*d))((x @ wqkv).reshape(B, S, 3, H, Dh), 2, 0, 3, 1, 4)
        q, k, v = qkv[0], qkv[1], qkv[2]
        scores = torch.einsum("bhqd,bhkd->bhqk", q, k) * (Dh ** -0.5)
        idx = torch.arange(S)
        mask = (idx[:, None] - idx[None, :] >= 0) & (idx[:, None] - idx[None, :] < W)
        scores = torch.where(mask[None, None], scores, -1e9)
        p = _softmax(scores, axis=-1)
        o = torch.einsum("bhqk,bhkd->bhqd", p, v)
        return (lambda x, *d: x.permute(*d))(o, 0, 2, 1, 3).reshape(B, S, D) @ wo

    def _B_ref(x, wqkv, wo):
        B, S, D = x.shape
        H = 16; Dh = D // H; W = 256
        qkv = (lambda x, *d: x.permute(*d))((x @ wqkv).reshape(B, S, 3, H, Dh), 2, 0, 3, 1, 4)
        q, k, v = qkv[0], qkv[1], qkv[2]
        scores = torch.einsum("bhqd,bhkd->bhqk", q, k) * (Dh ** -0.5)
        idx = torch.arange(S)
        mask = (idx[:, None] - idx[None, :] >= 0) & (idx[:, None] - idx[None, :] < W)
        scores = torch.where(mask[None, None], scores, -1e9)
        p = _softmax(scores, axis=-1)
        o = torch.einsum("bhqk,bhkd->bhqd", p, v)
        return (lambda x, *d: x.permute(*d))(o, 0, 2, 1, 3).reshape(B, S, D) @ wo

    a = _A_ref(A_x, A_wqkv, A_wo)
    b = _B_ref(B_x, B_wqkv, B_wo)
    return torch.concatenate([a, b], axis=-1)
```

```cpp
#include <hip/hip_runtime.h>
#include <hip/hip_bf16.h>
#define VARIANT 1
typedef __hip_bfloat16 bf16;
constexpr int S = 2048, D = 448, H = 16, DH = 28, W = 256, N3 = 1344;

__device__ __forceinline__ float bf2f(bf16 v) { return __bfloat162float(v); }
__device__ __forceinline__ float rbf(float v) { return __bfloat162float(__float2bfloat16(v)); }

__global__ void gemm_naive(const bf16* A, const bf16* B, bf16* C, int M, int N, int K, int ldc) {
    int n = blockIdx.x * 64 + (threadIdx.x & 63);
    int m = blockIdx.y * 4 + (threadIdx.x >> 6);
    if (n >= N || m >= M) return;
    float acc = 0.f;
    for (int k = 0; k < K; ++k) acc += bf2f(A[(size_t)m * K + k]) * bf2f(B[(size_t)k * N + n]);
    C[(size_t)m * ldc + n] = __float2bfloat16(acc);
}

__global__ void attn_naive(const bf16* qkv, bf16* o) {
    int q = blockIdx.x, h = blockIdx.y, lane = threadIdx.x;
    __shared__ float p[W];
    const bf16* qp = qkv + (size_t)q * N3 + h * DH;
    float qv[DH];
    for (int d = 0; d < DH; ++d) qv[d] = bf2f(qp[d]);
    const float scale = 0.1889822365046136f;
    float sc[4]; float mx = -1e30f;
    for (int i = 0; i < 4; ++i) {
        int j = q - (lane + 64 * i);
        float s = -1e9f;
        if (j >= 0) {
            const bf16* kp = qkv + (size_t)j * N3 + D + h * DH;
            float a = 0.f;
            for (int d = 0; d < DH; ++d) a += qv[d] * bf2f(kp[d]);
            a = a * scale;
            s = a;
        }
        sc[i] = s; mx = fmaxf(mx, s);
    }
    for (int m = 32; m >= 1; m >>= 1) mx = fmaxf(mx, __shfl_xor(mx, m));
    float sum = 0.f;
    for (int i = 0; i < 4; ++i) { sc[i] = __expf(sc[i] - mx); sum += sc[i]; }
    for (int m = 32; m >= 1; m >>= 1) sum += __shfl_xor(sum, m);
    for (int i = 0; i < 4; ++i) {
        p[lane + 64 * i] = rbf(sc[i]);
    }
    __syncthreads();
    if (lane < DH) {
        float a = 0.f;
        for (int i = 0; i < W; ++i) { int j = q - i; if (j >= 0) a += p[i] * bf2f(qkv[(size_t)j * N3 + 2 * D + h * DH + lane]); }
        a = a / sum;
        o[(size_t)q * D + h * DH + lane] = __float2bfloat16(a);
    }
}

extern "C" void kernel_launch(void* const* d_in, const int* in_sizes, int n_in, void* d_out, int out_size, void* d_ws, size_t ws_size, hipStream_t stream) {
    bf16* out = (bf16*)d_out;
    char* ws = (char*)d_ws;
    for (int b = 0; b < 2; ++b) {
        const bf16* x = (const bf16*)d_in[3 * b + 0];
        const bf16* wqkv = (const bf16*)d_in[3 * b + 1];
        const bf16* wo = (const bf16*)d_in[3 * b + 2];
        bf16* qkv = (bf16*)(ws + (size_t)b * 16 * 1024 * 1024);
        bf16* o = (bf16*)(ws + (size_t)b * 16 * 1024 * 1024 + 8 * 1024 * 1024);
        gemm_naive<<<dim3(N3 / 64, S / 4), 256, 0, stream>>>(x, wqkv, qkv, S, N3, D, N3);
        attn_naive<<<dim3(S, H), 64, 0, stream>>>(qkv, o);
        gemm_naive<<<dim3(D / 64, S / 4), 256, 0, stream>>>(o, wo, out + b * D, S, D, D, 2 * D);
    }
}
```

```cpp
#include <hip/hip_runtime.h>
#include <hip/hip_bf16.h>
#include <stdint.h>

typedef unsigned short u16;
typedef short bf16x8 __attribute__((ext_vector_type(8)));
typedef short s16x4 __attribute__((ext_vector_type(4)));
typedef short v4i16_t __attribute__((ext_vector_type(4)));
typedef float f32x16 __attribute__((ext_vector_type(16)));
typedef float f32x2 __attribute__((ext_vector_type(2)));
typedef __bf16 bf16x2_t __attribute__((ext_vector_type(2)));
typedef unsigned u32x4 __attribute__((ext_vector_type(4)));
#define LAS __attribute__((address_space(3)))
#define GAS __attribute__((address_space(1)))
typedef GAS unsigned long long gu64;
#define RLX_AGENT __ATOMIC_RELAXED, __HIP_MEMORY_SCOPE_AGENT
#define DI __device__ __forceinline__
#define MFMA(a, b, c) __builtin_amdgcn_mfma_f32_32x32x16_bf16((a), (b), (c), 0, 0, 0)
#define WAIT_BAR(N) asm volatile("s_waitcnt vmcnt(" #N ") lgkmcnt(0)\n\ts_barrier" ::: "memory")
#define LBAR() asm volatile("s_waitcnt lgkmcnt(0)\n\ts_barrier" ::: "memory")
#define VM0_VISIBLE() __builtin_amdgcn_s_waitcnt(0x0F70)

constexpr unsigned WS_GEN = 0, WS_KFLAG = 4096, WS_VFLAG = 8192, WS_K = 1u << 20, WS_V = 6u << 20, WS_O = 16u << 20, WS_BYTES = 24u << 20;
#define TS(k) do {} while (0)
constexpr float QSCALE = 0.1889822365046136f * 1.4426950408889634f;
constexpr unsigned SPIN_CAP = 1u << 18;
constexpr unsigned RSLOT = 24576, L_WV = 98304;
constexpr unsigned L_K = 65536, L_V = 114688;

DI void glds16s(const void* sbase, unsigned voff, unsigned lds_dst) {
    unsigned sv;
    asm volatile("s_mov_b32 %0, m0\n\ts_mov_b32 m0, %3\n\ts_nop 0\n\tglobal_load_lds_dwordx4 %1, %2\n\ts_mov_b32 m0, %0"
                 : "=&s"(sv) : "v"(voff), "s"(sbase), "s"(lds_dst) : "memory");
}
DI unsigned pk(float a, float b) { f32x2 v = {a, b}; bf16x2_t r = __builtin_convertvector(v, bf16x2_t); return __builtin_bit_cast(unsigned, r); }
DI bf16x8 ldsr(const LAS char* p) { return *(const LAS bf16x8*)p; }
DI s16x4 ldstr(const LAS char* p) { return __builtin_bit_cast(s16x4, __builtin_amdgcn_ds_read_tr16_b64_v4i16((LAS v4i16_t*)p)); }
DI bf16x8 ldstr2(const LAS char* p, int off2) { s16x4 a = ldstr(p), b = ldstr(p + off2); return __builtin_shufflevector(a, b, 0, 1, 2, 3, 4, 5, 6, 7); }
DI float max3(float a, float b, float c) { return __builtin_fmaxf(__builtin_fmaxf(a, b), c); }
DI float tmax(const f32x16& p, float m) {
    float a = max3(m, p[0], p[1]), b = max3(p[2], p[3], p[4]);
    a = max3(a, p[5], p[6]); b = max3(b, p[7], p[8]); a = max3(a, p[9], p[10]); b = max3(b, p[11], p[12]); a = max3(a, p[13], p[14]);
    return max3(a, b, p[15]);
}
DI float xhalf_max(float m) { auto rr = __builtin_amdgcn_permlane32_swap(__float_as_uint(m), __float_as_uint(m), false, false); return __builtin_fmaxf(__uint_as_float(rr[0]), __uint_as_float(rr[1])); }
DI u32x4 pk4(const f32x16& a, int s) { return (u32x4){pk(a[8 * s], a[8 * s + 1]), pk(a[8 * s + 2], a[8 * s + 3]), pk(a[8 * s + 4], a[8 * s + 5]), pk(a[8 * s + 6], a[8 * s + 7])}; }
#define BC8(x) __builtin_bit_cast(bf16x8, x)

DI void wait_flags(const gu64* fp, unsigned long long tag, unsigned long long need, unsigned long long first) {
    unsigned long long v = first;
    for (unsigned spins = 0; (__ballot(v == tag) & need) != need && spins <= SPIN_CAP; ++spins) {
        __builtin_amdgcn_s_sleep(1);
        v = __hip_atomic_load(fp, RLX_AGENT);
    }
}

__global__ void __launch_bounds__(512, 2)
fused_kernel(const u16* __restrict__ xA, const u16* __restrict__ wA, const u16* __restrict__ oA,
             const u16* __restrict__ xB, const u16* __restrict__ wB, const u16* __restrict__ oB,
             u16* __restrict__ out, char* ws)
{
    extern __shared__ __attribute__((aligned(16))) char lds_[];
    const LAS char* lds = (const LAS char*)lds_;
    LAS char* ldsw = (LAS char*)lds_;
    const unsigned lds0 = (unsigned)(uintptr_t)lds_;
    TS(0);
    const int tid = threadIdx.x, lane = tid & 63;
    const int w = __builtin_amdgcn_readfirstlane(tid >> 6);
    const int bid = blockIdx.x, xcd = bid & 7, idx = bid >> 3;
    const int b = xcd >> 2, g = idx & 7, j = (xcd & 3) * 4 + (idx >> 3);
    const int hh = w >> 2, tt = w & 3;
    const char* x = (const char*)(b ? xB : xA) + (size_t)j * 128 * 896;
    const char* wq = (const char*)(b ? wB : wA) + 112 * g;
#define ROT ((idx >> 3) * 2 + (idx & 7)) % 7
    const int rot = ROT;
    const unsigned xoff0 = (unsigned)((16 * w + (lane >> 3)) * 896 + (((lane & 7) ^ (lane >> 4)) * 16));
    const unsigned xoff1 = (unsigned)((16 * w + 8 + (lane >> 3)) * 896 + (((lane & 7) ^ (4 + (lane >> 4))) * 16));
    const unsigned woff = (unsigned)((lane >> 3) * 2688 + ((((lane >> 2) & 1) ^ ((lane >> 4) & 1)) * 48) + (lane & 3) * 16);
    auto kc_of = [&](int p) { int k = p + rot; return k >= 7 ? k - 7 : k; };
    auto issue_w = [&](int which, int p, unsigned dst) {
        glds16s(wq + (size_t)(64 * kc_of(p) + 8 * w) * 2688 + which * 896, woff, lds0 + dst + w * 1024);
    };
    auto issue_step = [&](int p) {
        const unsigned slot = (p & 3) * RSLOT;
        const char* xs = x + kc_of(p) * 128;
        glds16s(xs, xoff0, lds0 + slot + (16 * w) * 128);
        glds16s(xs, xoff1, lds0 + slot + (16 * w + 8) * 128);
        issue_w(1, p, slot + 16384);
    };
    issue_step(0); issue_step(1); issue_step(2); issue_step(3);
    TS(1);
    const char* wo = (const char*)(b ? oB : oA);
    const int r32 = lane & 31, hi = lane >> 5;
    const __amdgpu_buffer_rsrc_t wsr = __builtin_amdgcn_make_buffer_rsrc(ws, 0, WS_BYTES, 0x00020000);
    const int unit = (b * 8 + g) * 16 + j;
    const unsigned long long gen = __hip_atomic_load((gu64*)(ws + WS_GEN), RLX_AGENT);

    const int q4 = (lane & 15) >> 2, p4 = lane & 3, blk = (lane >> 4) & 1;
    unsigned xa[4];
#pragma unroll
    for (int s = 0; s < 4; ++s) xa[s] = (unsigned)((32 * tt + r32) * 128 + (((hi ^ ((r32 >> 1) & 7)) * 16) ^ (s * 32)));
    const unsigned wa = (unsigned)((8 * hi + q4) * 128 + ((hh ^ (q4 >> 1)) * 64) + 32 * blk + 8 * p4);
    const bool padk = hh ? (hi == 0) : (hi == 1);

    bf16x8 xf[7][4];
    f32x16 accK = {};
#define KSTEP(p) do { \
    _Pragma("unroll") for (int s = 0; s < 4; ++s) { \
        xf[p][s] = ldsr(lds + ((p) & 3) * RSLOT + xa[s]); \
        bf16x8 kf = ldstr2(lds + ((p) & 3) * RSLOT + 16384 + wa + s * 2048, 512); \
        accK = MFMA(kf, xf[p][s], accK); } } while (0)
    WAIT_BAR(9); TS(2); KSTEP(0);
    WAIT_BAR(6); issue_step(4); KSTEP(1);
    WAIT_BAR(6); issue_step(5); KSTEP(2);
    WAIT_BAR(6); issue_step(6); KSTEP(3);
    WAIT_BAR(6); issue_w(2, 0, L_WV); issue_w(2, 1, L_WV + 8192); issue_w(2, 2, L_WV + 2 * 8192); issue_w(2, 3, L_WV + 3 * 8192); KSTEP(4);
    WAIT_BAR(7); issue_w(2, 4, L_WV + 4 * 8192); issue_w(2, 5, L_WV + 5 * 8192); issue_w(2, 6, L_WV + 6 * 8192); KSTEP(5);
    WAIT_BAR(7); KSTEP(6);
    TS(3);
    const unsigned toff = (unsigned)(hh * 8192 + tt * 2048) + lane * 16;
    u32x4 k0, k1;
    {
#pragma unroll
        for (int r = 0; r < 4; ++r) { accK[r] = (padk && hh) ? 0.f : accK[r]; accK[12 + r] = (padk && !hh) ? 0.f : accK[12 + r]; }
        k0 = pk4(accK, 0); k1 = pk4(accK, 1);
        const unsigned gaddr = WS_K + (unsigned)unit * 16384u + toff;
        __builtin_amdgcn_raw_buffer_store_b128(k0, wsr, gaddr, 0, 16);
        __builtin_amdgcn_raw_buffer_store_b128(k1, wsr, gaddr + 1024, 0, 16);
        const unsigned oaddr = WS_O + (unsigned)(((b * 64 + 4 * j + tt) * 16 + 2 * g + hh) * 2048) + lane * 16;
        const u32x4 poison = {0xFFFFFFFFu, 0xFFFFFFFFu, 0xFFFFFFFFu, 0xFFFFFFFFu};
        __builtin_amdgcn_raw_buffer_store_b128(poison, wsr, oaddr, 0, 16);
        __builtin_amdgcn_raw_buffer_store_b128(poison, wsr, oaddr + 1024, 0, 16);
    }
    WAIT_BAR(7);
#pragma unroll
    for (int p = 0; p < 7; ++p) issue_w(0, p, p * 8192);
    f32x16 accV = {};
#define VSTEP(p) do { _Pragma("unroll") for (int s = 0; s < 4; ++s) { \
        bf16x8 vf = ldstr2(lds + L_WV + (p) * 8192 + wa + s * 2048, 512); accV = MFMA(xf[p][s], vf, accV); } } while (0)
    VSTEP(0); VSTEP(1); VSTEP(2); VSTEP(3);
    WAIT_BAR(7);
    const unsigned long long tag = gen + 1ull;
    if (tid == 0) __hip_atomic_store((gu64*)(ws + WS_KFLAG) + unit, tag, RLX_AGENT);
    TS(4);
    VSTEP(4); VSTEP(5); VSTEP(6);
    u32x4 v0, v1;
    {
        v0 = pk4(accV, 0); v1 = pk4(accV, 1);
        const bool padv = hh ? (r32 < 4) : (r32 >= 28);
        const unsigned fill = (r32 == (hh ? 0 : 28)) ? 0x3F803F80u : 0u;
        if (padv) { v0 = (u32x4){fill, fill, fill, fill}; v1 = v0; }
        const unsigned gaddr = WS_V + (unsigned)unit * 16384u + toff;
        __builtin_amdgcn_raw_buffer_store_b128(v0, wsr, gaddr, 0, 16);
        __builtin_amdgcn_raw_buffer_store_b128(v1, wsr, gaddr + 1024, 0, 16);
    }
    TS(5);
    WAIT_BAR(5);
    int fidx = unit;
    if (lane == 0) fidx = j >= 1 ? unit - 1 : unit;
    else if (lane == 1) fidx = j >= 2 ? unit - 2 : unit;
    else if (lane < 10) fidx = (b * 8 + (lane - 2)) * 16 + j;
    const gu64* kfp = (const gu64*)(ws + WS_KFLAG) + fidx;
    const gu64* vfp = (const gu64*)(ws + WS_VFLAG) + fidx;
    f32x16 accQ = {};
#define QSTEP(p) do { _Pragma("unroll") for (int s = 0; s < 4; ++s) { \
        bf16x8 qf = ldstr2(lds + (p) * 8192 + wa + s * 2048, 512); accQ = MFMA(qf, xf[p][s], accQ); } } while (0)
    QSTEP(0); QSTEP(1); QSTEP(2); QSTEP(3);
    WAIT_BAR(0);
    if (tid == 0) __hip_atomic_store((gu64*)(ws + WS_VFLAG) + unit, tag, RLX_AGENT);
    const unsigned long long kfl0 = __hip_atomic_load(kfp, RLX_AGENT);
    TS(6);
    QSTEP(4); QSTEP(5); QSTEP(6);
    const unsigned long long need = (j >= 2 ? 3ull : (j >= 1 ? 1ull : 0ull));
    wait_flags(kfp, tag, need | 0x3FCull, kfl0);
    TS(7);
    u32x4 hk[4];
    const u32x4 dumK0 = {0u, 0u, 0u, 0u};
    {
        const unsigned ga = WS_K + (unsigned)unit * 16384u + toff;
        if (j >= 2) { hk[0] = __builtin_amdgcn_raw_buffer_load_b128(wsr, ga - 32768, 0, 16); hk[1] = __builtin_amdgcn_raw_buffer_load_b128(wsr, ga - 32768 + 1024, 0, 16); }
        if (j >= 1) { hk[2] = __builtin_amdgcn_raw_buffer_load_b128(wsr, ga - 16384, 0, 16); hk[3] = __builtin_amdgcn_raw_buffer_load_b128(wsr, ga - 16384 + 1024, 0, 16); }
    }
    bf16x8 Q0, Q1;
    {
        accQ[0] = (padk && hh) ? 1.f : accQ[0]; accQ[12] = (padk && !hh) ? 1.f : accQ[12];
#pragma unroll
        for (int r = 1; r < 4; ++r) { accQ[r] = (padk && hh) ? 0.f : accQ[r]; accQ[12 + r] = (padk && !hh) ? 0.f : accQ[12 + r]; }
        Q0 = BC8(pk4(accQ, 0)); Q1 = BC8(pk4(accQ, 1));
    }
    const unsigned long long vfl0 = __hip_atomic_load(vfp, RLX_AGENT);
    LBAR();
    TS(8);
    const unsigned tl = (unsigned)(tt * 4096 + hh * 2048) + lane * 16;
    *(LAS u32x4*)(ldsw + L_K + 32768 + tl) = k0; *(LAS u32x4*)(ldsw + L_K + 32768 + tl + 1024) = k1;
    *(LAS u32x4*)(ldsw + L_V + 32768 + tl) = v0; *(LAS u32x4*)(ldsw + L_V + 32768 + tl + 1024) = v1;
    {
        const unsigned neg = 0xF149u;
        u32x4 d0 = dumK0, d1 = dumK0;
        if (hh) { d0[0] = hi == 0 ? neg : 0u; } else { d1[2] = hi == 1 ? neg : 0u; }
        if (j < 2) { hk[0] = d0; hk[1] = d1; }
        if (j < 1) { hk[2] = d0; hk[3] = d1; }
        *(LAS u32x4*)(ldsw + L_K + tl) = hk[0]; *(LAS u32x4*)(ldsw + L_K + tl + 1024) = hk[1];
        *(LAS u32x4*)(ldsw + L_K + 16384 + tl) = hk[2]; *(LAS u32x4*)(ldsw + L_K + 16384 + tl + 1024) = hk[3];
    }
    VM0_VISIBLE();
    LBAR();
    TS(9);
    const int cq = g >> 1, th = g & 1;
    const unsigned ooff = (unsigned)((lane >> 4) * 896 + min((lane & 15) ^ ((lane >> 4) << 2), 13) * 16);
    const char* wos = wo + 224 * cq;
    wait_flags(vfp, tag, need, vfl0);
    u32x4 hv[4];
    {
        const unsigned ga = WS_V + (unsigned)unit * 16384u + toff;
        if (j >= 2) { hv[0] = __builtin_amdgcn_raw_buffer_load_b128(wsr, ga - 32768, 0, 16); hv[1] = __builtin_amdgcn_raw_buffer_load_b128(wsr, ga - 32768 + 1024, 0, 16); }
        else { hv[0] = dumK0; hv[1] = dumK0; }
        if (j >= 1) { hv[2] = __builtin_amdgcn_raw_buffer_load_b128(wsr, ga - 16384, 0, 16); hv[3] = __builtin_amdgcn_raw_buffer_load_b128(wsr, ga - 16384 + 1024, 0, 16); }
        else { hv[2] = dumK0; hv[3] = dumK0; }
    }
    TS(10);
    const f32x16 zero16 = {};
    const LAS char* kl = lds + L_K + tl;
    const LAS char* vl = lds + L_V + tl;
    f32x16 S[9];
#pragma unroll
    for (int u = 8; u >= 0; --u) { bf16x8 a0 = ldsr(kl + u * 4096), a1 = ldsr(kl + u * 4096 + 1024); S[u] = MFMA(a0, Q0, zero16); S[u] = MFMA(a1, Q1, S[u]); }
    unsigned msk[8];
#pragma unroll
    for (int r = 0; r < 16; ++r) { const int key = (r & 3) + 8 * (r >> 2) + 4 * hi; S[8][r] = key <= r32 ? S[8][r] : S[0][r]; }
#pragma unroll
    for (int i = 0; i < 8; ++i) { const int key = ((2 * i) & 3) + 8 * ((2 * i) >> 2) + 4 * hi; msk[i] = (key <= r32 ? 0xFFFFu : 0u) | (key + 1 <= r32 ? 0xFFFF0000u : 0u); }
    float m = S[8][0];
#pragma unroll
    for (int u = 8; u >= 1; --u) m = tmax(S[u], m);
    m = xhalf_max(m) * QSCALE;
    TS(11);
    u32x4 P[9][2];
#define EXPT(u) do { _Pragma("unroll") for (int r = 0; r < 16; ++r) S[u][r] = __builtin_amdgcn_exp2f(__builtin_fmaf(S[u][r], QSCALE, -m)); \
        P[u][0] = pk4(S[u], 0); P[u][1] = pk4(S[u], 1); } while (0)
    EXPT(8); EXPT(7); EXPT(6); EXPT(5);
#pragma unroll
    for (int i = 0; i < 4; ++i) { P[0][0][i] = P[8][0][i] & ~msk[i]; P[0][1][i] = P[8][1][i] & ~msk[4 + i]; P[8][0][i] &= msk[i]; P[8][1][i] &= msk[4 + i]; }
    *(LAS u32x4*)(ldsw + L_V + tl) = hv[0]; *(LAS u32x4*)(ldsw + L_V + tl + 1024) = hv[1];
    *(LAS u32x4*)(ldsw + L_V + 16384 + tl) = hv[2]; *(LAS u32x4*)(ldsw + L_V + 16384 + tl + 1024) = hv[3];
    VM0_VISIBLE();
    LBAR();
    TS(12);
#pragma unroll
    for (int i = 0; i < 8; ++i) glds16s(wos + (size_t)(32 * w + 4 * i) * 896, ooff, lds0 + (32 * w + 4 * i) * 256);
    f32x16 O = {};
#define PVT(u) do { bf16x8 a0 = ldsr(vl + (u) * 4096), a1 = ldsr(vl + (u) * 4096 + 1024); O = MFMA(a0, BC8(P[u][0]), O); O = MFMA(a1, BC8(P[u][1]), O); } while (0)
    PVT(8); EXPT(4); PVT(7); EXPT(3); PVT(6); EXPT(2); PVT(5); EXPT(1); PVT(4); PVT(3); PVT(2); PVT(1); PVT(0);
    TS(13);
    {
        const float lsrc = hh ? O[0] : O[12];
        auto rr = __builtin_amdgcn_permlane32_swap(__float_as_uint(lsrc), __float_as_uint(lsrc), false, false);
        const float l = hh ? __uint_as_float(rr[0]) : __uint_as_float(rr[1]);
        const float inv = __builtin_amdgcn_rcpf(l);
#pragma unroll
        for (int r = 0; r < 16; ++r) O[r] *= inv;
#pragma unroll
        for (int r = 0; r < 4; ++r) { O[r] = (padk && hh) ? 0.f : O[r]; O[12 + r] = (padk && !hh) ? 0.f : O[12 + r]; }
        u32x4 o0 = pk4(O, 0), o1 = pk4(O, 1);
#pragma unroll
        for (int i = 0; i < 4; ++i) { o0[i] = min(o0[i], 0xFFFFFFFEu); o1[i] = min(o1[i], 0xFFFFFFFEu); }
        const unsigned oaddr = WS_O + (unsigned)(((b * 64 + 4 * j + tt) * 16 + 2 * g + hh) * 2048) + lane * 16;
        __builtin_amdgcn_raw_buffer_store_b128(o0, wsr, oaddr, 0, 16);
        __builtin_amdgcn_raw_buffer_store_b128(o1, wsr, oaddr + 1024, 0, 16);
    }
    WAIT_BAR(2);
    TS(14);
#pragma unroll
    for (int i = 0; i < 6; ++i) glds16s(wos + (size_t)(256 + 24 * w + 4 * i) * 896, ooff, lds0 + (256 + 24 * w + 4 * i) * 256);
    u32x4 of[8];
    const unsigned ga = WS_O + (unsigned)(((b * 64 + 4 * j + 2 * th) * 16 + 2 * w) * 2048) + lane * 16;
#pragma unroll
    for (int i = 0; i < 4; ++i) { of[i] = __builtin_amdgcn_raw_buffer_load_b128(wsr, ga + i * 1024, 0, 16); of[4 + i] = __builtin_amdgcn_raw_buffer_load_b128(wsr, ga + 32768 + i * 1024, 0, 16); }
    const int t2 = w & 1, nt = w >> 1;
    const int no = nt == 3 ? 80 : 32 * nt;
    const unsigned aa = (unsigned)((4 * hi + q4) * 256 + (((no >> 3) + 2 * blk + (p4 >> 1)) ^ (q4 << 2)) * 16 + 8 * (p4 & 1));
    bf16x8 af[32];
#define AFRAG(h, s) af[2 * (h) + (s)] = ldstr2(lds + aa + (28 * (h) - 4 * ((h) & 1) + 16 * (s)) * 256, 8 * 256)
#pragma unroll
    for (int h = 0; h < 9; ++h) { AFRAG(h, 0); AFRAG(h, 1); }
    const s16x4 a9lo = ldstr(lds + aa + 248 * 256);
    LBAR();
    TS(15);
    for (unsigned spins = 0;; ++spins) {
        unsigned mx = 0;
#pragma unroll
        for (int i = 0; i < 8; ++i) mx = max(max(mx, max(of[i][0], of[i][1])), max(of[i][2], of[i][3]));
        if (__all(mx != 0xFFFFFFFFu) || spins > SPIN_CAP) break;
#pragma unroll
        for (int i = 0; i < 4; ++i) { of[i] = __builtin_amdgcn_raw_buffer_load_b128(wsr, ga + i * 1024, 0, 16); of[4 + i] = __builtin_amdgcn_raw_buffer_load_b128(wsr, ga + 32768 + i * 1024, 0, 16); }
    }
#pragma unroll
    for (int i = 0; i < 4; ++i) {
        *(LAS u32x4*)(ldsw + (2 * w) * 2048 + i * 1024 + lane * 16) = of[i];
        *(LAS u32x4*)(ldsw + 32768 + (2 * w) * 2048 + i * 1024 + lane * 16) = of[4 + i];
    }
    TS(16);
    WAIT_BAR(0);
    TS(17);
    f32x16 acc = {};
    const LAS char* ol = lds + t2 * 32768 + lane * 16;
    {
        bf16x8 bq[18];
#pragma unroll
        for (int i = 0; i < 18; ++i) bq[i] = ldsr(ol + i * 1024);
        { const s16x4 a9hi = ldstr(lds + aa + 256 * 256); af[18] = __builtin_shufflevector(a9lo, a9hi, 0, 1, 2, 3, 4, 5, 6, 7); }
        AFRAG(9, 1);
#pragma unroll
        for (int h = 10; h < 16; ++h) { AFRAG(h, 0); AFRAG(h, 1); }
#pragma unroll
        for (int i = 0; i < 18; ++i) acc = MFMA(af[i], bq[i], acc);
    }
    {
        bf16x8 bq[14];
#pragma unroll
        for (int i = 0; i < 14; ++i) bq[i] = ldsr(ol + (18 + i) * 1024);
#pragma unroll
        for (int i = 0; i < 14; ++i) acc = MFMA(af[18 + i], bq[i], acc);
    }
    TS(18);
    {
        u16* orow = out + (size_t)(128 * j + 64 * th + 32 * t2 + r32) * 896 + 448 * b + 112 * cq + no + (hi ? 8 : 0);
#pragma unroll
        for (int pr = 0; pr < 2; ++pr) {
            unsigned ax = pk(acc[8 * pr], acc[8 * pr + 1]), ay = pk(acc[8 * pr + 2], acc[8 * pr + 3]);
            unsigned bx = pk(acc[8 * pr + 4], acc[8 * pr + 5]), by = pk(acc[8 * pr + 6], acc[8 * pr + 7]);
            auto rx = __builtin_amdgcn_permlane32_swap(ax, bx, false, false);
            auto ry = __builtin_amdgcn_permlane32_swap(ay, by, false, false);
            if (!(nt == 3 && pr == 0)) *(u32x4*)(orow + 16 * pr) = (u32x4){rx[0], ry[0], rx[1], ry[1]};
        }
    }
    if (bid == 0 && w == 7) {
        const gu64* fp = (const gu64*)(ws + WS_KFLAG) + lane * 4;
        for (unsigned spins = 0; spins <= SPIN_CAP; ++spins) {
            bool ok = true;
#pragma unroll
            for (int i = 0; i < 4; ++i) ok &= __hip_atomic_load(fp + i, RLX_AGENT) == tag;
            if (__all(ok)) break;
            __builtin_amdgcn_s_sleep(1);
        }
        if (lane == 0) __hip_atomic_store((gu64*)(ws + WS_GEN), tag, RLX_AGENT);
    }
}

extern "C" void kernel_launch(void* const* d_in, const int* in_sizes, int n_in, void* d_out, int out_size, void* d_ws, size_t ws_size, hipStream_t stream) {
    (void)in_sizes; (void)n_in; (void)out_size; (void)ws_size;
    (void)hipFuncSetAttribute((const void*)fused_kernel, hipFuncAttributeMaxDynamicSharedMemorySize, 160 * 1024);
    fused_kernel<<<256, 512, 160 * 1024, stream>>>((const u16*)d_in[0], (const u16*)d_in[1], (const u16*)d_in[2], (const u16*)d_in[3], (const u16*)d_in[4], (const u16*)d_in[5], (u16*)d_out, (char*)d_ws);
}
```

```cpp
#include <hip/hip_runtime.h>
#include <hip/hip_bf16.h>
#include <stdint.h>

typedef unsigned short u16;
typedef short bf16x8 __attribute__((ext_vector_type(8)));
typedef short s16x4 __attribute__((ext_vector_type(4)));
typedef short v4i16_t __attribute__((ext_vector_type(4)));
typedef float f32x16 __attribute__((ext_vector_type(16)));
typedef float f32x2 __attribute__((ext_vector_type(2)));
typedef __bf16 bf16x2_t __attribute__((ext_vector_type(2)));
typedef unsigned u32x4 __attribute__((ext_vector_type(4)));
#define LAS __attribute__((address_space(3)))
#define GAS __attribute__((address_space(1)))
typedef GAS unsigned long long gu64;
#define RLX_AGENT __ATOMIC_RELAXED, __HIP_MEMORY_SCOPE_AGENT
#define DI __device__ __forceinline__
#define MFMA(a, b, c) __builtin_amdgcn_mfma_f32_32x32x16_bf16((a), (b), (c), 0, 0, 0)
#define LBAR() asm volatile("s_waitcnt lgkmcnt(0)\n\ts_barrier" ::: "memory")
#define SYNC(N) do { if (isL) asm volatile("s_waitcnt vmcnt(" #N ")" ::: "memory"); asm volatile("s_waitcnt lgkmcnt(0)\n\ts_barrier" ::: "memory"); } while (0)
#define VM0_VISIBLE() __builtin_amdgcn_s_waitcnt(0x0F70)

constexpr unsigned WS_GEN = 0, WS_KFLAG = 4096, WS_VFLAG = 8192, WS_K = 1u << 20, WS_V = 6u << 20, WS_O = 16u << 20, WS_BYTES = 24u << 20;
#define TS(k) do {} while (0)
constexpr float QSCALE = 0.1889822365046136f * 1.4426950408889634f;
constexpr unsigned SPIN_CAP = 1u << 18;
constexpr unsigned RSLOT = 24576, L_WV = 98304, L_STK = 73728, L_STV = 57344, L_CNT = 155648, L_CNTV = 69632;
constexpr unsigned L_KV = 65536;

DI void glds16s(const void* sbase, unsigned voff, unsigned lds_dst) {
    unsigned sv;
    asm volatile("s_mov_b32 %0, m0\n\ts_mov_b32 m0, %3\n\ts_nop 0\n\tglobal_load_lds_dwordx4 %1, %2\n\ts_mov_b32 m0, %0"
                 : "=&s"(sv) : "v"(voff), "s"(sbase), "s"(lds_dst) : "memory");
}
DI void glds16s_sc1(const void* sbase, unsigned voff, unsigned lds_dst) {
    unsigned sv;
    asm volatile("s_mov_b32 %0, m0\n\ts_mov_b32 m0, %3\n\ts_nop 0\n\tglobal_load_lds_dwordx4 %1, %2 sc1\n\ts_mov_b32 m0, %0"
                 : "=&s"(sv) : "v"(voff), "s"(sbase), "s"(lds_dst) : "memory");
}
DI unsigned pk(float a, float b) { f32x2 v = {a, b}; bf16x2_t r = __builtin_convertvector(v, bf16x2_t); return __builtin_bit_cast(unsigned, r); }
DI bf16x8 ldsr(const LAS char* p) { return *(const LAS bf16x8*)p; }
DI u32x4 ldsu(const LAS char* p) { return *(const LAS u32x4*)p; }
DI s16x4 ldstr(const LAS char* p) { return __builtin_bit_cast(s16x4, __builtin_amdgcn_ds_read_tr16_b64_v4i16((LAS v4i16_t*)p)); }
DI bf16x8 ldstr2(const LAS char* p, int off2) { s16x4 a = ldstr(p), b = ldstr(p + off2); return __builtin_shufflevector(a, b, 0, 1, 2, 3, 4, 5, 6, 7); }
DI float max3(float a, float b, float c) { return __builtin_fmaxf(__builtin_fmaxf(a, b), c); }
DI float tmax(const f32x16& p, float m) {
    float a = max3(m, p[0], p[1]), b = max3(p[2], p[3], p[4]);
    a = max3(a, p[5], p[6]); b = max3(b, p[7], p[8]); a = max3(a, p[9], p[10]); b = max3(b, p[11], p[12]); a = max3(a, p[13], p[14]);
    return max3(a, b, p[15]);
}
DI float xhalf_max(float m) { auto rr = __builtin_amdgcn_permlane32_swap(__float_as_uint(m), __float_as_uint(m), false, false); return __builtin_fmaxf(__uint_as_float(rr[0]), __uint_as_float(rr[1])); }
DI u32x4 pk4(const f32x16& a, int s) { return (u32x4){pk(a[8 * s], a[8 * s + 1]), pk(a[8 * s + 2], a[8 * s + 3]), pk(a[8 * s + 4], a[8 * s + 5]), pk(a[8 * s + 6], a[8 * s + 7])}; }
#define BC8(x) __builtin_bit_cast(bf16x8, x)

DI void gld_o(u32x4 (&r)[8], const char* sbase, unsigned va, unsigned vb) {
    asm volatile("global_load_dwordx4 %0, %8, %10 sc1\n\tglobal_load_dwordx4 %1, %8, %10 offset:1024 sc1\n\t"
                 "global_load_dwordx4 %2, %8, %10 offset:2048 sc1\n\tglobal_load_dwordx4 %3, %8, %10 offset:3072 sc1\n\t"
                 "global_load_dwordx4 %4, %9, %10 sc1\n\tglobal_load_dwordx4 %5, %9, %10 offset:1024 sc1\n\t"
                 "global_load_dwordx4 %6, %9, %10 offset:2048 sc1\n\tglobal_load_dwordx4 %7, %9, %10 offset:3072 sc1\n\t"
                 "s_waitcnt vmcnt(0)"
                 : "=&v"(r[0]), "=&v"(r[1]), "=&v"(r[2]), "=&v"(r[3]), "=&v"(r[4]), "=&v"(r[5]), "=&v"(r[6]), "=&v"(r[7])
                 : "v"(va), "v"(vb), "s"(sbase) : "memory");
}
DI unsigned long long gld8_wait(const char* sbase, unsigned voff) {
    unsigned long long r;
    asm volatile("global_load_dwordx2 %0, %1, %2 sc1\n\ts_waitcnt vmcnt(0)" : "=&v"(r) : "v"(voff), "s"(sbase) : "memory");
    return r;
}
DI void wait_flags(const char* sbase, unsigned voff, unsigned long long tag, unsigned long long need) {
    for (unsigned spins = 0; spins <= SPIN_CAP; ++spins) {
        const unsigned long long v = gld8_wait(sbase, voff);
        if ((__ballot(v == tag) & need) == need) break;
        __builtin_amdgcn_s_sleep(1);
    }
}

__global__ void __launch_bounds__(512, 2)
fused_kernel(const u16* __restrict__ xA, const u16* __restrict__ wA, const u16* __restrict__ oA,
             const u16* __restrict__ xB, const u16* __restrict__ wB, const u16* __restrict__ oB,
             u16* __restrict__ out, char* ws)
{
    extern __shared__ __attribute__((aligned(16))) char lds_[];
    const LAS char* lds = (const LAS char*)lds_;
    LAS char* ldsw = (LAS char*)lds_;
    const unsigned lds0 = (unsigned)(uintptr_t)lds_;
    TS(0);
    const int tid = threadIdx.x, lane = tid & 63;
    const int w = __builtin_amdgcn_readfirstlane(tid >> 6);
    const int bid = blockIdx.x, xcd = bid & 7, idx = bid >> 3;
    const int b = xcd >> 2, g = idx & 7, j = (xcd & 3) * 4 + (idx >> 3);
    const int hh = w >> 2, tt = w & 3;
    const bool isL = w < 4;
    const int c4 = w & 3;
    const char* x = (const char*)(b ? xB : xA) + (size_t)j * 128 * 896;
    const char* wq = (const char*)(b ? wB : wA) + 112 * g;
#define ROT ((idx >> 3) * 2 + (idx & 7)) % 7
    const int rot = ROT;
    const unsigned xoffA = (unsigned)((32 * c4 + (lane >> 3)) * 896 + (((lane & 7) ^ (lane >> 4)) * 16));
    const unsigned xoffB = (unsigned)((32 * c4 + 8 + (lane >> 3)) * 896 + (((lane & 7) ^ (4 + (lane >> 4))) * 16));
    const unsigned woff = (unsigned)((lane >> 3) * 2688 + ((((lane >> 2) & 1) ^ ((lane >> 4) & 1)) * 48) + (lane & 3) * 16);
    auto kc_of = [&](int p) { int k = p + rot; return k >= 7 ? k - 7 : k; };
    auto issue_w = [&](int which, int p, unsigned dst) {
        const char* src = wq + (size_t)(64 * kc_of(p) + 16 * c4) * 2688 + which * 896;
        glds16s(src, woff, lds0 + dst + c4 * 2048);
        glds16s(src + 8 * 2688, woff, lds0 + dst + c4 * 2048 + 1024);
    };
    auto issue_step = [&](int p) {
        const unsigned slot = (p & 3) * RSLOT;
        const char* xs = x + kc_of(p) * 128;
        glds16s(xs, xoffA, lds0 + slot + (32 * c4) * 128);
        glds16s(xs, xoffB, lds0 + slot + (32 * c4 + 8) * 128);
        glds16s(xs + 16 * 896, xoffA, lds0 + slot + (32 * c4 + 16) * 128);
        glds16s(xs + 16 * 896, xoffB, lds0 + slot + (32 * c4 + 24) * 128);
        issue_w(1, p, slot + 16384);
    };
    unsigned long long tag;
    if (isL) { issue_step(0); issue_step(1); issue_step(2); issue_step(3); }
    else {
        asm volatile("global_load_dwordx2 %0, %1, %2 sc1" : "=v"(tag) : "v"(0u), "s"(ws + WS_GEN) : "memory");
        if (tid == 256) *(LAS unsigned long long*)(ldsw + L_CNT) = 0ull;
        {
            unsigned long long pc;
            asm volatile("s_getpc_b64 %0" : "=s"(pc));
            const char* cb = (const char*)(pc & ~127ull);
            unsigned sv;
#pragma unroll
            for (int i = 0; i < 1; ++i)
                asm volatile("s_mov_b32 %0, m0\n\ts_mov_b32 m0, %3\n\ts_nop 0\n\tglobal_load_lds_dword %1, %2\n\ts_mov_b32 m0, %0"
                             : "=&s"(sv) : "v"((unsigned)(min(c4 * 64 + lane, 199) * 128)), "s"(cb), "s"(lds0 + L_CNT + 256 + c4 * 256) : "memory");
        }
    }
    TS(1);
    const char* wo = (const char*)(b ? oB : oA);
    const int r32 = lane & 31, hi = lane >> 5;
    const __amdgpu_buffer_rsrc_t wsr = __builtin_amdgcn_make_buffer_rsrc(ws, 0, WS_BYTES, 0x00020000);
    const int unit = (b * 8 + g) * 16 + j;

    const int q4 = (lane & 15) >> 2, p4 = lane & 3, blk = (lane >> 4) & 1;
    unsigned xa[4];
#pragma unroll
    for (int s = 0; s < 4; ++s) xa[s] = (unsigned)((32 * tt + r32) * 128 + (((hi ^ ((r32 >> 1) & 7)) * 16) ^ (s * 32)));
    const unsigned wa = (unsigned)((8 * hi + q4) * 128 + ((hh ^ (q4 >> 1)) * 64) + 32 * blk + 8 * p4);
    const bool padk = hh ? (hi == 0) : (hi == 1);
    const unsigned toff = (unsigned)(hh * 8192 + tt * 2048) + lane * 16;
    const unsigned coff = (unsigned)(c4 * 4096) + lane * 16;

    bf16x8 xf[7][4];
    f32x16 accK = {};
#define KSTEP(p) do { \
    _Pragma("unroll") for (int s = 0; s < 4; ++s) { \
        xf[p][s] = ldsr(lds + ((p) & 3) * RSLOT + xa[s]); \
        bf16x8 kf = ldstr2(lds + ((p) & 3) * RSLOT + 16384 + wa + s * 2048, 512); \
        accK = MFMA(kf, xf[p][s], accK); } } while (0)
    SYNC(18); TS(2); KSTEP(0);
    SYNC(12); if (isL) issue_step(4); KSTEP(1);
    SYNC(12); if (isL) issue_step(5); KSTEP(2);
    SYNC(12); if (isL) issue_step(6); KSTEP(3);
    SYNC(12); KSTEP(4);
    SYNC(6); if (isL) { issue_w(2, 0, L_WV); issue_w(2, 1, L_WV + 8192); } KSTEP(5);
    SYNC(4); if (isL) { issue_w(2, 2, L_WV + 2 * 8192); issue_w(2, 3, L_WV + 3 * 8192); } KSTEP(6);
    TS(3);
    u32x4 k0, k1;
    {
#pragma unroll
        for (int r = 0; r < 4; ++r) { accK[r] = (padk && hh) ? 0.f : accK[r]; accK[12 + r] = (padk && !hh) ? 0.f : accK[12 + r]; }
        k0 = pk4(accK, 0); k1 = pk4(accK, 1);
        *(LAS u32x4*)(ldsw + L_STK + toff) = k0; *(LAS u32x4*)(ldsw + L_STK + toff + 1024) = k1;
    }
    SYNC(4);
    if (isL) { issue_w(2, 4, L_WV + 4 * 8192); issue_w(2, 5, L_WV + 5 * 8192); issue_w(2, 6, L_WV + 6 * 8192); }
    else {
        const unsigned gaddr = WS_K + (unsigned)unit * 16384u + coff;
#pragma unroll
        for (int i = 0; i < 4; ++i) __builtin_amdgcn_raw_buffer_store_b128(ldsu(lds + L_STK + coff + i * 1024), wsr, gaddr + i * 1024, 0, 16);
        const unsigned oaddr = WS_O + (unsigned)(((b * 64 + 4 * j + c4) * 16 + 2 * g) * 2048) + lane * 16;
        const u32x4 poison = {0xFFFFFFFFu, 0xFFFFFFFFu, 0xFFFFFFFFu, 0xFFFFFFFFu};
#pragma unroll
        for (int i = 0; i < 4; ++i) __builtin_amdgcn_raw_buffer_store_b128(poison, wsr, oaddr + i * 1024, 0, 16);
    }
    f32x16 accV = {};
#define VSTEP(p) do { _Pragma("unroll") for (int s = 0; s < 4; ++s) { \
        bf16x8 vf = ldstr2(lds + L_WV + (p) * 8192 + wa + s * 2048, 512); accV = MFMA(xf[p][s], vf, accV); } } while (0)
    VSTEP(0); VSTEP(1);
    SYNC(6);
    if (isL) { issue_w(0, 0, 0); issue_w(0, 1, 8192); issue_w(0, 2, 2 * 8192); }
    VSTEP(2); VSTEP(3);
    SYNC(6);
    if (isL) { issue_w(0, 3, 3 * 8192); issue_w(0, 4, 4 * 8192); }
    VSTEP(4); VSTEP(5); VSTEP(6);
    TS(4);
    u32x4 v0, v1;
    {
        v0 = pk4(accV, 0); v1 = pk4(accV, 1);
        const bool padv = hh ? (r32 < 4) : (r32 >= 28);
        const unsigned fill = (r32 == (hh ? 0 : 28)) ? 0x3F803F80u : 0u;
        if (padv) { v0 = (u32x4){fill, fill, fill, fill}; v1 = v0; }
        *(LAS u32x4*)(ldsw + L_STV + toff) = v0; *(LAS u32x4*)(ldsw + L_STV + toff + 1024) = v1;
    }
    SYNC(4);
    if (isL) { issue_w(0, 5, 5 * 8192); issue_w(0, 6, 6 * 8192); }
    else {
        const unsigned gaddr = WS_V + (unsigned)unit * 16384u + coff;
#pragma unroll
        for (int i = 0; i < 4; ++i) __builtin_amdgcn_raw_buffer_store_b128(ldsu(lds + L_STV + coff + i * 1024), wsr, gaddr + i * 1024, 0, 16);
        if (tid == 448) *(LAS unsigned*)(ldsw + L_CNTV) = 0u;
    }
    f32x16 accQ = {};
#define QSTEP(p) do { _Pragma("unroll") for (int s = 0; s < 4; ++s) { \
        bf16x8 qf = ldstr2(lds + (p) * 8192 + wa + s * 2048, 512); accQ = MFMA(qf, xf[p][s], accQ); } } while (0)
    QSTEP(0); QSTEP(1); QSTEP(2);
    if (!isL) {
        asm volatile("s_waitcnt vmcnt(4)" : "+v"(tag) :: "memory");
        tag += 1ull;
        unsigned old = 0;
        if (lane == 0) old = __hip_atomic_fetch_add((LAS unsigned*)(ldsw + L_CNT), 1u, __ATOMIC_RELAXED, __HIP_MEMORY_SCOPE_WORKGROUP);
        if (__builtin_amdgcn_readfirstlane(old) == 3u && lane == 0) __hip_atomic_store((gu64*)(ws + WS_KFLAG) + unit, tag, RLX_AGENT);
    }
    TS(5);
    SYNC(4);
    QSTEP(3); QSTEP(4);
    SYNC(0);
    QSTEP(5); QSTEP(6);
    TS(6);
    bf16x8 Q0, Q1;
    {
        accQ[0] = (padk && hh) ? 1.f : accQ[0]; accQ[12] = (padk && !hh) ? 1.f : accQ[12];
#pragma unroll
        for (int r = 1; r < 4; ++r) { accQ[r] = (padk && hh) ? 0.f : accQ[r]; accQ[12 + r] = (padk && !hh) ? 0.f : accQ[12 + r]; }
        Q0 = BC8(pk4(accQ, 0)); Q1 = BC8(pk4(accQ, 1));
    }
    LBAR();
    const int cq = g >> 1, th = g & 1;
    const unsigned ooff = (unsigned)((lane >> 4) * 896 + min((lane & 15) ^ ((lane >> 4) << 2), 13) * 16);
    const char* wos = wo + 224 * cq;
    const unsigned tl = (unsigned)(tt * 8192 + hh * 2048) + lane * 16;
    *(LAS u32x4*)(ldsw + L_KV + 65536 + tl) = k0; *(LAS u32x4*)(ldsw + L_KV + 65536 + tl + 1024) = k1;
    *(LAS u32x4*)(ldsw + L_KV + 65536 + 4096 + tl) = v0; *(LAS u32x4*)(ldsw + L_KV + 65536 + 4096 + tl + 1024) = v1;
    int fidx = unit;
    if (lane == 0) fidx = j >= 1 ? unit - 1 : unit;
    else if (lane == 1) fidx = j >= 2 ? unit - 2 : unit;
    else if (lane < 10) fidx = (b * 8 + (lane - 2)) * 16 + j;
    const unsigned long long need = (j >= 2 ? 3ull : (j >= 1 ? 1ull : 0ull));
    const unsigned hdst = (unsigned)((2 * (c4 & 1)) * 8192 + (c4 >> 1) * 2048);
    if (isL) {
#pragma unroll
        for (int i = 0; i < 8; ++i) glds16s(wos + (size_t)(64 * c4 + 4 * i) * 896, ooff, lds0 + (64 * c4 + 4 * i) * 256);
    } else {
        asm volatile("s_waitcnt vmcnt(0)" ::: "memory");
        unsigned old = 0;
        if (lane == 0) old = __hip_atomic_fetch_add((LAS unsigned*)(ldsw + L_CNTV), 1u, __ATOMIC_RELAXED, __HIP_MEMORY_SCOPE_WORKGROUP);
        if (__builtin_amdgcn_readfirstlane(old) == 3u && lane == 0) __hip_atomic_store((gu64*)(ws + WS_VFLAG) + unit, tag, RLX_AGENT);
        wait_flags(ws + WS_KFLAG, fidx * 8, tag, need | 0x3FCull);
        TS(7);
        const unsigned neg = 0xF149u;
        const bool odd = (c4 >> 1) != 0;
        const u32x4 d0 = {(odd && hi == 0) ? neg : 0u, 0u, 0u, 0u}, d1 = {0u, 0u, (!odd && hi == 1) ? neg : 0u, 0u};
#pragma unroll
        for (int i = 0; i < 4; ++i) {
            const unsigned o = hdst + (i >> 1) * 8192 + (i & 1) * 1024;
            if (j >= 2) glds16s_sc1(ws + WS_K + (size_t)(unit - 2) * 16384 + i * 1024, coff, lds0 + L_KV + o);
            else *(LAS u32x4*)(ldsw + L_KV + o + lane * 16) = (i & 1) ? d1 : d0;
            if (j >= 1) glds16s_sc1(ws + WS_K + (size_t)(unit - 1) * 16384 + i * 1024, coff, lds0 + L_KV + 32768 + o);
            else *(LAS u32x4*)(ldsw + L_KV + 32768 + o + lane * 16) = (i & 1) ? d1 : d0;
        }
        asm volatile("s_waitcnt vmcnt(0)" ::: "memory");
    }
    LBAR();
    TS(8);
    if (isL) {
#pragma unroll
        for (int i = 8; i < 16; ++i) glds16s(wos + (size_t)(64 * c4 + 4 * i) * 896, ooff, lds0 + (64 * c4 + 4 * i) * 256);
    }
    const f32x16 zero16 = {};
    const LAS char* kl = lds + L_KV + tl;
    const LAS char* vl = lds + L_KV + 4096 + tl;
    f32x16 S[9];
#pragma unroll
    for (int u = 8; u >= 0; --u) { bf16x8 a0 = ldsr(kl + u * 8192), a1 = ldsr(kl + u * 8192 + 1024); S[u] = MFMA(a0, Q0, zero16); S[u] = MFMA(a1, Q1, S[u]); }
    unsigned msk[8];
#pragma unroll
    for (int r = 0; r < 16; ++r) { const int key = (r & 3) + 8 * (r >> 2) + 4 * hi; S[8][r] = key <= r32 ? S[8][r] : S[0][r]; }
#pragma unroll
    for (int i = 0; i < 8; ++i) { const int key = ((2 * i) & 3) + 8 * ((2 * i) >> 2) + 4 * hi; msk[i] = (key <= r32 ? 0xFFFFu : 0u) | (key + 1 <= r32 ? 0xFFFF0000u : 0u); }
    float m = S[8][0];
#pragma unroll
    for (int u = 8; u >= 1; --u) m = tmax(S[u], m);
    m = xhalf_max(m) * QSCALE;
    TS(9);
    if (!isL) {
        wait_flags(ws + WS_VFLAG, fidx * 8, tag, need);
        const u32x4 z = {0u, 0u, 0u, 0u};
#pragma unroll
        for (int i = 0; i < 4; ++i) {
            const unsigned o = 4096 + hdst + (i >> 1) * 8192 + (i & 1) * 1024;
            if (j >= 2) glds16s_sc1(ws + WS_V + (size_t)(unit - 2) * 16384 + i * 1024, coff, lds0 + L_KV + o);
            else *(LAS u32x4*)(ldsw + L_KV + o + lane * 16) = z;
            if (j >= 1) glds16s_sc1(ws + WS_V + (size_t)(unit - 1) * 16384 + i * 1024, coff, lds0 + L_KV + 32768 + o);
            else *(LAS u32x4*)(ldsw + L_KV + 32768 + o + lane * 16) = z;
        }
    }
    TS(10);
    u32x4 P[9][2];
#define EXPT(u) do { _Pragma("unroll") for (int r = 0; r < 16; ++r) S[u][r] = __builtin_amdgcn_exp2f(__builtin_fmaf(S[u][r], QSCALE, -m)); \
        P[u][0] = pk4(S[u], 0); P[u][1] = pk4(S[u], 1); } while (0)
    EXPT(8); EXPT(7); EXPT(6); EXPT(5);
#pragma unroll
    for (int i = 0; i < 4; ++i) { P[0][0][i] = P[8][0][i] & ~msk[i]; P[0][1][i] = P[8][1][i] & ~msk[4 + i]; P[8][0][i] &= msk[i]; P[8][1][i] &= msk[4 + i]; }
    if (!isL) asm volatile("s_waitcnt vmcnt(0)" ::: "memory");
    LBAR();
    TS(11);
    f32x16 O = {};
#define PVT(u) do { bf16x8 a0 = ldsr(vl + (u) * 8192), a1 = ldsr(vl + (u) * 8192 + 1024); O = MFMA(a0, BC8(P[u][0]), O); O = MFMA(a1, BC8(P[u][1]), O); } while (0)
    PVT(8); EXPT(4); PVT(7); EXPT(3); PVT(6); EXPT(2); PVT(5); EXPT(1); PVT(4); PVT(3); PVT(2); PVT(1); PVT(0);
    TS(12);
    {
        const float lsrc = hh ? O[0] : O[12];
        auto rr = __builtin_amdgcn_permlane32_swap(__float_as_uint(lsrc), __float_as_uint(lsrc), false, false);
        const float l = hh ? __uint_as_float(rr[0]) : __uint_as_float(rr[1]);
        const float inv = __builtin_amdgcn_rcpf(l);
#pragma unroll
        for (int r = 0; r < 16; ++r) O[r] *= inv;
#pragma unroll
        for (int r = 0; r < 4; ++r) { O[r] = (padk && hh) ? 0.f : O[r]; O[12 + r] = (padk && !hh) ? 0.f : O[12 + r]; }
        u32x4 o0 = pk4(O, 0), o1 = pk4(O, 1);
#pragma unroll
        for (int i = 0; i < 4; ++i) { o0[i] = min(o0[i], 0xFFFFFFFEu); o1[i] = min(o1[i], 0xFFFFFFFEu); }
        if (isL) asm volatile("s_waitcnt vmcnt(0)" ::: "memory");
        const unsigned oaddr = WS_O + (unsigned)(((b * 64 + 4 * j + tt) * 16 + 2 * g + hh) * 2048) + lane * 16;
        __builtin_amdgcn_raw_buffer_store_b128(o0, wsr, oaddr, 0, 16);
        __builtin_amdgcn_raw_buffer_store_b128(o1, wsr, oaddr + 1024, 0, 16);
    }
    LBAR();
    TS(13);
    u32x4 of[8];
    const unsigned ga = WS_O + (unsigned)(((b * 64 + 4 * j + 2 * th) * 16 + 2 * w) * 2048) + lane * 16;
    if (isL) {
#pragma unroll
        for (int i = 0; i < 12; ++i) glds16s(wos + (size_t)(256 + 48 * c4 + 4 * i) * 896, ooff, lds0 + (256 + 48 * c4 + 4 * i) * 256);
    }
    const int t2 = w & 1, nt = w >> 1;
    const int no = nt == 3 ? 80 : 32 * nt;
    const unsigned aa = (unsigned)((4 * hi + q4) * 256 + (((no >> 3) + 2 * blk + (p4 >> 1)) ^ (q4 << 2)) * 16 + 8 * (p4 & 1));
    bf16x8 af[32];
#define AFRAG(h, s) af[2 * (h) + (s)] = ldstr2(lds + aa + (28 * (h) - 4 * ((h) & 1) + 16 * (s)) * 256, 8 * 256)
#pragma unroll
    for (int h = 0; h < 9; ++h) { AFRAG(h, 0); AFRAG(h, 1); }
    const s16x4 a9lo = ldstr(lds + aa + 248 * 256);
    LBAR();
    TS(14);
    for (unsigned spins = 0;; ++spins) {
        gld_o(of, ws, ga, ga + 32768);
        unsigned mx = 0;
#pragma unroll
        for (int i = 0; i < 8; ++i) mx = max(max(mx, max(of[i][0], of[i][1])), max(of[i][2], of[i][3]));
        if (__all(mx != 0xFFFFFFFFu) || spins > SPIN_CAP) break;
    }
#pragma unroll
    for (int i = 0; i < 4; ++i) {
        *(LAS u32x4*)(ldsw + (2 * w) * 2048 + i * 1024 + lane * 16) = of[i];
        *(LAS u32x4*)(ldsw + 32768 + (2 * w) * 2048 + i * 1024 + lane * 16) = of[4 + i];
    }
    TS(15);
    asm volatile("s_waitcnt vmcnt(0) lgkmcnt(0)\n\ts_barrier" ::: "memory");
    TS(16);
    f32x16 acc = {};
    const LAS char* ol = lds + t2 * 32768 + lane * 16;
    {
        bf16x8 bq[18];
#pragma unroll
        for (int i = 0; i < 18; ++i) bq[i] = ldsr(ol + i * 1024);
        { const s16x4 a9hi = ldstr(lds + aa + 256 * 256); af[18] = __builtin_shufflevector(a9lo, a9hi, 0, 1, 2, 3, 4, 5, 6, 7); }
        AFRAG(9, 1);
#pragma unroll
        for (int h = 10; h < 16; ++h) { AFRAG(h, 0); AFRAG(h, 1); }
#pragma unroll
        for (int i = 0; i < 18; ++i) acc = MFMA(af[i], bq[i], acc);
    }
    {
        bf16x8 bq[14];
#pragma unroll
        for (int i = 0; i < 14; ++i) bq[i] = ldsr(ol + (18 + i) * 1024);
#pragma unroll
        for (int i = 0; i < 14; ++i) acc = MFMA(af[18 + i], bq[i], acc);
    }
    TS(17);
    {
        u16* orow = out + (size_t)(128 * j + 64 * th + 32 * t2 + r32) * 896 + 448 * b + 112 * cq + no + (hi ? 8 : 0);
#pragma unroll
        for (int pr = 0; pr < 2; ++pr) {
            unsigned ax = pk(acc[8 * pr], acc[8 * pr + 1]), ay = pk(acc[8 * pr + 2], acc[8 * pr + 3]);
            unsigned bx = pk(acc[8 * pr + 4], acc[8 * pr + 5]), by = pk(acc[8 * pr + 6], acc[8 * pr + 7]);
            auto rx = __builtin_amdgcn_permlane32_swap(ax, bx, false, false);
            auto ry = __builtin_amdgcn_permlane32_swap(ay, by, false, false);
            if (!(nt == 3 && pr == 0)) *(u32x4*)(orow + 16 * pr) = (u32x4){rx[0], ry[0], rx[1], ry[1]};
        }
    }
    if (bid == 0 && w == 7) {
        for (unsigned spins = 0; spins <= SPIN_CAP; ++spins) {
            bool ok = true;
#pragma unroll
            for (int i = 0; i < 4; ++i) ok &= gld8_wait(ws + WS_KFLAG, lane * 32 + i * 8) == tag;
            if (__all(ok)) break;
            __builtin_amdgcn_s_sleep(1);
        }
        if (lane == 0) __hip_atomic_store((gu64*)(ws + WS_GEN), tag, RLX_AGENT);
    }
}

extern "C" void kernel_launch(void* const* d_in, const int* in_sizes, int n_in, void* d_out, int out_size, void* d_ws, size_t ws_size, hipStream_t stream) {
    (void)in_sizes; (void)n_in; (void)out_size; (void)ws_size;
    (void)hipFuncSetAttribute((const void*)fused_kernel, hipFuncAttributeMaxDynamicSharedMemorySize, 160 * 1024);
    fused_kernel<<<256, 512, 160 * 1024, stream>>>((const u16*)d_in[0], (const u16*)d_in[1], (const u16*)d_in[2], (const u16*)d_in[3], (const u16*)d_in[4], (const u16*)d_in[5], (u16*)d_out, (char*)d_ws);
}
```

```cpp
#include <hip/hip_runtime.h>
#include <hip/hip_bf16.h>
#include <stdint.h>

typedef unsigned short u16;
typedef short bf16x8 __attribute__((ext_vector_type(8)));
typedef short s16x4 __attribute__((ext_vector_type(4)));
typedef short v4i16_t __attribute__((ext_vector_type(4)));
typedef float f32x16 __attribute__((ext_vector_type(16)));
typedef float f32x2 __attribute__((ext_vector_type(2)));
typedef __bf16 bf16x2_t __attribute__((ext_vector_type(2)));
typedef unsigned u32x4 __attribute__((ext_vector_type(4)));
#define LAS __attribute__((address_space(3)))
#define GAS __attribute__((address_space(1)))
typedef GAS unsigned long long gu64;
#define RLX_AGENT __ATOMIC_RELAXED, __HIP_MEMORY_SCOPE_AGENT
#define DI __device__ __forceinline__
#define MFMA(a, b, c) __builtin_amdgcn_mfma_f32_32x32x16_bf16((a), (b), (c), 0, 0, 0)
#define LBAR() asm volatile("s_waitcnt lgkmcnt(0)\n\ts_barrier" ::: "memory")
#define SYNC(N) do { if (isL) asm volatile("s_waitcnt vmcnt(" #N ")" ::: "memory"); asm volatile("s_waitcnt lgkmcnt(0)\n\ts_barrier" ::: "memory"); } while (0)
#define VM0_VISIBLE() __builtin_amdgcn_s_waitcnt(0x0F70)

constexpr unsigned WS_GEN = 0, WS_KFLAG = 4096, WS_VFLAG = 8192, WS_K = 1u << 20, WS_V = 6u << 20, WS_O = 16u << 20, WS_BYTES = 24u << 20;
#define TS(k) do {} while (0)
constexpr float QSCALE = 0.1889822365046136f * 1.4426950408889634f;
constexpr unsigned SPIN_CAP = 1u << 18;
constexpr unsigned RSLOT = 24576, L_WV = 98304, L_STK = 73728, L_STV = 57344, L_CNT = 155648, L_CNTV = 69632;
constexpr unsigned L_KV = 65536;

DI void glds16s(const void* sbase, unsigned voff, unsigned lds_dst) {
    unsigned sv;
    asm volatile("s_mov_b32 %0, m0\n\ts_mov_b32 m0, %3\n\ts_nop 0\n\tglobal_load_lds_dwordx4 %1, %2\n\ts_mov_b32 m0, %0"
                 : "=&s"(sv) : "v"(voff), "s"(sbase), "s"(lds_dst) : "memory");
}
DI void glds16s_sc1(const void* sbase, unsigned voff, unsigned lds_dst) {
    unsigned sv;
    asm volatile("s_mov_b32 %0, m0\n\ts_mov_b32 m0, %3\n\ts_nop 0\n\tglobal_load_lds_dwordx4 %1, %2 sc1\n\ts_mov_b32 m0, %0"
                 : "=&s"(sv) : "v"(voff), "s"(sbase), "s"(lds_dst) : "memory");
}
DI unsigned pk(float a, float b) { f32x2 v = {a, b}; bf16x2_t r = __builtin_convertvector(v, bf16x2_t); return __builtin_bit_cast(unsigned, r); }
DI bf16x8 ldsr(const LAS char* p) { return *(const LAS bf16x8*)p; }
DI u32x4 ldsu(const LAS char* p) { return *(const LAS u32x4*)p; }
DI s16x4 ldstr(const LAS char* p) { return __builtin_bit_cast(s16x4, __builtin_amdgcn_ds_read_tr16_b64_v4i16((LAS v4i16_t*)p)); }
DI bf16x8 ldstr2(const LAS char* p, int off2) { s16x4 a = ldstr(p), b = ldstr(p + off2); return __builtin_shufflevector(a, b, 0, 1, 2, 3, 4, 5, 6, 7); }
DI float max3(float a, float b, float c) { return __builtin_fmaxf(__builtin_fmaxf(a, b), c); }
DI float tmax(const f32x16& p, float m) {
    float a = max3(m, p[0], p[1]), b = max3(p[2], p[3], p[4]);
    a = max3(a, p[5], p[6]); b = max3(b, p[7], p[8]); a = max3(a, p[9], p[10]); b = max3(b, p[11], p[12]); a = max3(a, p[13], p[14]);
    return max3(a, b, p[15]);
}
DI float xhalf_max(float m) { auto rr = __builtin_amdgcn_permlane32_swap(__float_as_uint(m), __float_as_uint(m), false, false); return __builtin_fmaxf(__uint_as_float(rr[0]), __uint_as_float(rr[1])); }
DI u32x4 pk4(const f32x16& a, int s) { return (u32x4){pk(a[8 * s], a[8 * s + 1]), pk(a[8 * s + 2], a[8 * s + 3]), pk(a[8 * s + 4], a[8 * s + 5]), pk(a[8 * s + 6], a[8 * s + 7])}; }
#define BC8(x) __builtin_bit_cast(bf16x8, x)

DI void gld_o(u32x4 (&r)[8], const char* sbase, unsigned va, unsigned vb) {
    asm volatile("global_load_dwordx4 %0, %8, %10 sc1\n\tglobal_load_dwordx4 %1, %8, %10 offset:1024 sc1\n\t"
                 "global_load_dwordx4 %2, %8, %10 offset:2048 sc1\n\tglobal_load_dwordx4 %3, %8, %10 offset:3072 sc1\n\t"
                 "global_load_dwordx4 %4, %9, %10 sc1\n\tglobal_load_dwordx4 %5, %9, %10 offset:1024 sc1\n\t"
                 "global_load_dwordx4 %6, %9, %10 offset:2048 sc1\n\tglobal_load_dwordx4 %7, %9, %10 offset:3072 sc1\n\t"
                 "s_waitcnt vmcnt(0)"
                 : "=&v"(r[0]), "=&v"(r[1]), "=&v"(r[2]), "=&v"(r[3]), "=&v"(r[4]), "=&v"(r[5]), "=&v"(r[6]), "=&v"(r[7])
                 : "v"(va), "v"(vb), "s"(sbase) : "memory");
}
DI unsigned long long gld8_wait(const char* sbase, unsigned voff) {
    unsigned long long r;
    asm volatile("global_load_dwordx2 %0, %1, %2 sc1\n\ts_waitcnt vmcnt(0)" : "=&v"(r) : "v"(voff), "s"(sbase) : "memory");
    return r;
}
DI void wait_flags(const char* sbase, unsigned voff, unsigned long long tag, unsigned long long need) {
    for (unsigned spins = 0; spins <= SPIN_CAP; ++spins) {
        const unsigned long long v = gld8_wait(sbase, voff);
        if ((__ballot(v == tag) & need) == need) break;
        __builtin_amdgcn_s_sleep(1);
    }
}

__global__ void __launch_bounds__(512, 2)
fused_kernel(const u16* __restrict__ xA, const u16* __restrict__ wA, const u16* __restrict__ oA,
             const u16* __restrict__ xB, const u16* __restrict__ wB, const u16* __restrict__ oB,
             u16* __restrict__ out, char* ws)
{
    extern __shared__ __attribute__((aligned(16))) char lds_[];
    const LAS char* lds = (const LAS char*)lds_;
    LAS char* ldsw = (LAS char*)lds_;
    const unsigned lds0 = (unsigned)(uintptr_t)lds_;
    TS(0);
    const int tid = threadIdx.x, lane = tid & 63;
    const int w = __builtin_amdgcn_readfirstlane(tid >> 6);
    const int bid = blockIdx.x, xcd = bid & 7, idx = bid >> 3;
    const int b = xcd >> 2, g = idx & 7, j = (xcd & 3) * 4 + (idx >> 3);
    const int hh = w >> 2, tt = w & 3;
    const bool isL = w < 4;
    const int c4 = w & 3;
    const char* x = (const char*)(b ? xB : xA) + (size_t)j * 128 * 896;
    const char* wq = (const char*)(b ? wB : wA) + 112 * g;
#define ROT ((idx >> 3) * 2 + (idx & 7)) % 7
    const int rot = ROT;
    const unsigned xoffA = (unsigned)((32 * c4 + (lane >> 3)) * 896 + (((lane & 7) ^ (lane >> 4)) * 16));
    const unsigned xoffB = (unsigned)((32 * c4 + 8 + (lane >> 3)) * 896 + (((lane & 7) ^ (4 + (lane >> 4))) * 16));
    const unsigned woff = (unsigned)((lane >> 3) * 2688 + ((((lane >> 2) & 1) ^ ((lane >> 4) & 1)) * 48) + (lane & 3) * 16);
    auto kc_of = [&](int p) { int k = p + rot; return k >= 7 ? k - 7 : k; };
    auto issue_w = [&](int which, int p, unsigned dst) {
        const char* src = wq + (size_t)(64 * kc_of(p) + 16 * c4) * 2688 + which * 896;
        glds16s(src, woff, lds0 + dst + c4 * 2048);
        glds16s(src + 8 * 2688, woff, lds0 + dst + c4 * 2048 + 1024);
    };
    auto issue_step = [&](int p) {
        const unsigned slot = (p & 3) * RSLOT;
        const char* xs = x + kc_of(p) * 128;
        glds16s(xs, xoffA, lds0 + slot + (32 * c4) * 128);
        glds16s(xs, xoffB, lds0 + slot + (32 * c4 + 8) * 128);
        glds16s(xs + 16 * 896, xoffA, lds0 + slot + (32 * c4 + 16) * 128);
        glds16s(xs + 16 * 896, xoffB, lds0 + slot + (32 * c4 + 24) * 128);
        issue_w(1, p, slot + 16384);
    };
    unsigned long long tag;
    if (isL) { issue_step(0); issue_step(1); issue_step(2); issue_step(3); }
    else {
        asm volatile("global_load_dwordx2 %0, %1, %2 sc1" : "=v"(tag) : "v"(0u), "s"(ws + WS_GEN) : "memory");
        if (tid == 256) *(LAS unsigned long long*)(ldsw + L_CNT) = 0ull;
        if (w == 5) {
            const int unit_ = (b * 8 + g) * 16 + j;
            const char* wo_ = (const char*)(b ? oB : oA);
            const char* p = ws + WS_KFLAG;
            const int u1 = j >= 1 ? unit_ - 1 : unit_, u2 = j >= 2 ? unit_ - 2 : unit_;
            if (lane == 0) p = ws + WS_K + (size_t)unit_ * 16384;
            if (lane == 1) p = ws + WS_K + (size_t)u1 * 16384;
            if (lane == 2) p = ws + WS_K + (size_t)u2 * 16384;
            if (lane == 3) p = ws + WS_V + (size_t)unit_ * 16384;
            if (lane == 4) p = ws + WS_V + (size_t)u1 * 16384;
            if (lane == 5) p = ws + WS_V + (size_t)u2 * 16384;
            if (lane == 6) p = ws + WS_O + (size_t)(((b * 64 + 4 * j) * 16 + 2 * g) * 2048);
            if (lane == 7) p = ws + WS_O + (size_t)(((b * 64 + 4 * j + 3) * 16 + 15) * 2048);
            if (lane == 8) p = ws + WS_VFLAG;
            if (lane == 9) p = wo_;
            if (lane == 10) p = wo_ + 224 * 896;
            if (lane == 11) p = wo_ + 447 * 896;
            if (lane == 12) p = (const char*)(out + (size_t)(128 * j) * 896);
            if (lane == 13) p = (const char*)(out + (size_t)(128 * j + 127) * 896);
            unsigned sv;
            asm volatile("s_mov_b32 %0, m0\n\ts_mov_b32 m0, %2\n\ts_nop 0\n\tglobal_load_lds_dword %1, off\n\ts_mov_b32 m0, %0"
                         : "=&s"(sv) : "v"(p), "s"(lds0 + L_CNT + 256) : "memory");
        }
    }
    TS(1);
    const char* wo = (const char*)(b ? oB : oA);
    const int r32 = lane & 31, hi = lane >> 5;
    const __amdgpu_buffer_rsrc_t wsr = __builtin_amdgcn_make_buffer_rsrc(ws, 0, WS_BYTES, 0x00020000);
    const int unit = (b * 8 + g) * 16 + j;

    const int q4 = (lane & 15) >> 2, p4 = lane & 3, blk = (lane >> 4) & 1;
    unsigned xa[4];
#pragma unroll
    for (int s = 0; s < 4; ++s) xa[s] = (unsigned)((32 * tt + r32) * 128 + (((hi ^ ((r32 >> 1) & 7)) * 16) ^ (s * 32)));
    const unsigned wa = (unsigned)((8 * hi + q4) * 128 + ((hh ^ (q4 >> 1)) * 64) + 32 * blk + 8 * p4);
    const bool padk = hh ? (hi == 0) : (hi == 1);
    const unsigned toff = (unsigned)(hh * 8192 + tt * 2048) + lane * 16;
    const unsigned coff = (unsigned)(c4 * 4096) + lane * 16;

    bf16x8 xf[7][4];
    f32x16 accK = {};
#define KSTEP(p) do { \
    _Pragma("unroll") for (int s = 0; s < 4; ++s) { \
        xf[p][s] = ldsr(lds + ((p) & 3) * RSLOT + xa[s]); \
        bf16x8 kf = ldstr2(lds + ((p) & 3) * RSLOT + 16384 + wa + s * 2048, 512); \
        accK = MFMA(kf, xf[p][s], accK); } } while (0)
    SYNC(18); TS(2); KSTEP(0);
    SYNC(12); if (isL) issue_step(4); KSTEP(1);
    SYNC(12); if (isL) issue_step(5); KSTEP(2);
    SYNC(12); if (isL) issue_step(6); KSTEP(3);
    SYNC(12); KSTEP(4);
    SYNC(6); if (isL) { issue_w(2, 0, L_WV); issue_w(2, 1, L_WV + 8192); } KSTEP(5);
    SYNC(4); if (isL) { issue_w(2, 2, L_WV + 2 * 8192); issue_w(2, 3, L_WV + 3 * 8192); } KSTEP(6);
    TS(3);
    u32x4 k0, k1;
    {
#pragma unroll
        for (int r = 0; r < 4; ++r) { accK[r] = (padk && hh) ? 0.f : accK[r]; accK[12 + r] = (padk && !hh) ? 0.f : accK[12 + r]; }
        k0 = pk4(accK, 0); k1 = pk4(accK, 1);
        *(LAS u32x4*)(ldsw + L_STK + toff) = k0; *(LAS u32x4*)(ldsw + L_STK + toff + 1024) = k1;
    }
    SYNC(4);
    if (isL) { issue_w(2, 4, L_WV + 4 * 8192); issue_w(2, 5, L_WV + 5 * 8192); issue_w(2, 6, L_WV + 6 * 8192); }
    else {
        const unsigned gaddr = WS_K + (unsigned)unit * 16384u + coff;
#pragma unroll
        for (int i = 0; i < 4; ++i) __builtin_amdgcn_raw_buffer_store_b128(ldsu(lds + L_STK + coff + i * 1024), wsr, gaddr + i * 1024, 0, 16);
        const unsigned oaddr = WS_O + (unsigned)(((b * 64 + 4 * j + c4) * 16 + 2 * g) * 2048) + lane * 16;
        const u32x4 poison = {0xFFFFFFFFu, 0xFFFFFFFFu, 0xFFFFFFFFu, 0xFFFFFFFFu};
#pragma unroll
        for (int i = 0; i < 4; ++i) __builtin_amdgcn_raw_buffer_store_b128(poison, wsr, oaddr + i * 1024, 0, 16);
    }
    f32x16 accV = {};
#define VSTEP(p) do { _Pragma("unroll") for (int s = 0; s < 4; ++s) { \
        bf16x8 vf = ldstr2(lds + L_WV + (p) * 8192 + wa + s * 2048, 512); accV = MFMA(xf[p][s], vf, accV); } } while (0)
    VSTEP(0); VSTEP(1);
    SYNC(6);
    if (isL) { issue_w(0, 0, 0); issue_w(0, 1, 8192); issue_w(0, 2, 2 * 8192); }
    VSTEP(2); VSTEP(3);
    SYNC(6);
    if (isL) { issue_w(0, 3, 3 * 8192); issue_w(0, 4, 4 * 8192); }
    VSTEP(4); VSTEP(5); VSTEP(6);
    TS(4);
    u32x4 v0, v1;
    {
        v0 = pk4(accV, 0); v1 = pk4(accV, 1);
        const bool padv = hh ? (r32 < 4) : (r32 >= 28);
        const unsigned fill = (r32 == (hh ? 0 : 28)) ? 0x3F803F80u : 0u;
        if (padv) { v0 = (u32x4){fill, fill, fill, fill}; v1 = v0; }
        *(LAS u32x4*)(ldsw + L_STV + toff) = v0; *(LAS u32x4*)(ldsw + L_STV + toff + 1024) = v1;
    }
    SYNC(4);
    if (isL) { issue_w(0, 5, 5 * 8192); issue_w(0, 6, 6 * 8192); }
    else {
        const unsigned gaddr = WS_V + (unsigned)unit * 16384u + coff;
#pragma unroll
        for (int i = 0; i < 4; ++i) __builtin_amdgcn_raw_buffer_store_b128(ldsu(lds + L_STV + coff + i * 1024), wsr, gaddr + i * 1024, 0, 16);
        if (tid == 448) *(LAS unsigned*)(ldsw + L_CNTV) = 0u;
    }
    f32x16 accQ = {};
#define QSTEP(p) do { _Pragma("unroll") for (int s = 0; s < 4; ++s) { \
        bf16x8 qf = ldstr2(lds + (p) * 8192 + wa + s * 2048, 512); accQ = MFMA(qf, xf[p][s], accQ); } } while (0)
    QSTEP(0); QSTEP(1); QSTEP(2);
    if (!isL) {
        asm volatile("s_waitcnt vmcnt(4)" : "+v"(tag) :: "memory");
        tag += 1ull;
        unsigned old = 0;
        if (lane == 0) old = __hip_atomic_fetch_add((LAS unsigned*)(ldsw + L_CNT), 1u, __ATOMIC_RELAXED, __HIP_MEMORY_SCOPE_WORKGROUP);
        if (__builtin_amdgcn_readfirstlane(old) == 3u && lane == 0) __hip_atomic_store((gu64*)(ws + WS_KFLAG) + unit, tag, RLX_AGENT);
    }
    TS(5);
    SYNC(4);
    QSTEP(3); QSTEP(4);
    SYNC(0);
    QSTEP(5); QSTEP(6);
    TS(6);
    bf16x8 Q0, Q1;
    {
        accQ[0] = (padk && hh) ? 1.f : accQ[0]; accQ[12] = (padk && !hh) ? 1.f : accQ[12];
#pragma unroll
        for (int r = 1; r < 4; ++r) { accQ[r] = (padk && hh) ? 0.f : accQ[r]; accQ[12 + r] = (padk && !hh) ? 0.f : accQ[12 + r]; }
        Q0 = BC8(pk4(accQ, 0)); Q1 = BC8(pk4(accQ, 1));
    }
    LBAR();
    const int cq = g >> 1, th = g & 1;
    const unsigned ooff = (unsigned)((lane >> 4) * 896 + min((lane & 15) ^ ((lane >> 4) << 2), 13) * 16);
    const char* wos = wo + 224 * cq;
    const unsigned tl = (unsigned)(tt * 8192 + hh * 2048) + lane * 16;
    *(LAS u32x4*)(ldsw + L_KV + 65536 + tl) = k0; *(LAS u32x4*)(ldsw + L_KV + 65536 + tl + 1024) = k1;
    *(LAS u32x4*)(ldsw + L_KV + 65536 + 4096 + tl) = v0; *(LAS u32x4*)(ldsw + L_KV + 65536 + 4096 + tl + 1024) = v1;
    int fidx = unit;
    if (lane == 0) fidx = j >= 1 ? unit - 1 : unit;
    else if (lane == 1) fidx = j >= 2 ? unit - 2 : unit;
    else if (lane < 10) fidx = (b * 8 + (lane - 2)) * 16 + j;
    const unsigned long long need = (j >= 2 ? 3ull : (j >= 1 ? 1ull : 0ull));
    const unsigned hdst = (unsigned)((2 * (c4 & 1)) * 8192 + (c4 >> 1) * 2048);
    if (isL) {
#pragma unroll
        for (int i = 0; i < 8; ++i) glds16s(wos + (size_t)(64 * c4 + 4 * i) * 896, ooff, lds0 + (64 * c4 + 4 * i) * 256);
    } else {
        asm volatile("s_waitcnt vmcnt(0)" ::: "memory");
        unsigned old = 0;
        if (lane == 0) old = __hip_atomic_fetch_add((LAS unsigned*)(ldsw + L_CNTV), 1u, __ATOMIC_RELAXED, __HIP_MEMORY_SCOPE_WORKGROUP);
        if (__builtin_amdgcn_readfirstlane(old) == 3u && lane == 0) __hip_atomic_store((gu64*)(ws + WS_VFLAG) + unit, tag, RLX_AGENT);
        wait_flags(ws + WS_KFLAG, fidx * 8, tag, need | 0x3FCull);
        TS(7);
        const unsigned neg = 0xF149u;
        const bool odd = (c4 >> 1) != 0;
        const u32x4 d0 = {(odd && hi == 0) ? neg : 0u, 0u, 0u, 0u}, d1 = {0u, 0u, (!odd && hi == 1) ? neg : 0u, 0u};
#pragma unroll
        for (int i = 0; i < 4; ++i) {
            const unsigned o = hdst + (i >> 1) * 8192 + (i & 1) * 1024;
            if (j >= 2) glds16s_sc1(ws + WS_K + (size_t)(unit - 2) * 16384 + i * 1024, coff, lds0 + L_KV + o);
            else *(LAS u32x4*)(ldsw + L_KV + o + lane * 16) = (i & 1) ? d1 : d0;
            if (j >= 1) glds16s_sc1(ws + WS_K + (size_t)(unit - 1) * 16384 + i * 1024, coff, lds0 + L_KV + 32768 + o);
            else *(LAS u32x4*)(ldsw + L_KV + 32768 + o + lane * 16) = (i & 1) ? d1 : d0;
        }
        asm volatile("s_waitcnt vmcnt(0)" ::: "memory");
    }
    LBAR();
    TS(8);
    if (isL) {
#pragma unroll
        for (int i = 8; i < 16; ++i) glds16s(wos + (size_t)(64 * c4 + 4 * i) * 896, ooff, lds0 + (64 * c4 + 4 * i) * 256);
    }
    const f32x16 zero16 = {};
    const LAS char* kl = lds + L_KV + tl;
    const LAS char* vl = lds + L_KV + 4096 + tl;
    f32x16 S[9];
#pragma unroll
    for (int u = 8; u >= 0; --u) { bf16x8 a0 = ldsr(kl + u * 8192), a1 = ldsr(kl + u * 8192 + 1024); S[u] = MFMA(a0, Q0, zero16); S[u] = MFMA(a1, Q1, S[u]); }
    unsigned msk[8];
#pragma unroll
    for (int r = 0; r < 16; ++r) { const int key = (r & 3) + 8 * (r >> 2) + 4 * hi; S[8][r] = key <= r32 ? S[8][r] : S[0][r]; }
#pragma unroll
    for (int i = 0; i < 8; ++i) { const int key = ((2 * i) & 3) + 8 * ((2 * i) >> 2) + 4 * hi; msk[i] = (key <= r32 ? 0xFFFFu : 0u) | (key + 1 <= r32 ? 0xFFFF0000u : 0u); }
    float m = S[8][0];
#pragma unroll
    for (int u = 8; u >= 1; --u) m = tmax(S[u], m);
    m = xhalf_max(m) * QSCALE;
    TS(9);
    if (!isL) {
        wait_flags(ws + WS_VFLAG, fidx * 8, tag, need);
        const u32x4 z = {0u, 0u, 0u, 0u};
#pragma unroll
        for (int i = 0; i < 4; ++i) {
            const unsigned o = 4096 + hdst + (i >> 1) * 8192 + (i & 1) * 1024;
            if (j >= 2) glds16s_sc1(ws + WS_V + (size_t)(unit - 2) * 16384 + i * 1024, coff, lds0 + L_KV + o);
            else *(LAS u32x4*)(ldsw + L_KV + o + lane * 16) = z;
            if (j >= 1) glds16s_sc1(ws + WS_V + (size_t)(unit - 1) * 16384 + i * 1024, coff, lds0 + L_KV + 32768 + o);
            else *(LAS u32x4*)(ldsw + L_KV + 32768 + o + lane * 16) = z;
        }
    }
    TS(10);
    u32x4 P[9][2];
#define EXPT(u) do { _Pragma("unroll") for (int r = 0; r < 16; ++r) S[u][r] = __builtin_amdgcn_exp2f(__builtin_fmaf(S[u][r], QSCALE, -m)); \
        P[u][0] = pk4(S[u], 0); P[u][1] = pk4(S[u], 1); } while (0)
    EXPT(8); EXPT(7); EXPT(6); EXPT(5);
#pragma unroll
    for (int i = 0; i < 4; ++i) { P[0][0][i] = P[8][0][i] & ~msk[i]; P[0][1][i] = P[8][1][i] & ~msk[4 + i]; P[8][0][i] &= msk[i]; P[8][1][i] &= msk[4 + i]; }
    if (!isL) asm volatile("s_waitcnt vmcnt(0)" ::: "memory");
    LBAR();
    TS(11);
    f32x16 O = {};
#define PVT(u) do { bf16x8 a0 = ldsr(vl + (u) * 8192), a1 = ldsr(vl + (u) * 8192 + 1024); O = MFMA(a0, BC8(P[u][0]), O); O = MFMA(a1, BC8(P[u][1]), O); } while (0)
    PVT(8); EXPT(4); PVT(7); EXPT(3); PVT(6); EXPT(2); PVT(5); EXPT(1); PVT(4); PVT(3); PVT(2); PVT(1); PVT(0);
    TS(12);
    {
        const float lsrc = hh ? O[0] : O[12];
        auto rr = __builtin_amdgcn_permlane32_swap(__float_as_uint(lsrc), __float_as_uint(lsrc), false, false);
        const float l = hh ? __uint_as_float(rr[0]) : __uint_as_float(rr[1]);
        const float inv = __builtin_amdgcn_rcpf(l);
#pragma unroll
        for (int r = 0; r < 16; ++r) O[r] *= inv;
#pragma unroll
        for (int r = 0; r < 4; ++r) { O[r] = (padk && hh) ? 0.f : O[r]; O[12 + r] = (padk && !hh) ? 0.f : O[12 + r]; }
        u32x4 o0 = pk4(O, 0), o1 = pk4(O, 1);
#pragma unroll
        for (int i = 0; i < 4; ++i) { o0[i] = min(o0[i], 0xFFFFFFFEu); o1[i] = min(o1[i], 0xFFFFFFFEu); }
        if (isL) asm volatile("s_waitcnt vmcnt(0)" ::: "memory");
        const unsigned oaddr = WS_O + (unsigned)(((b * 64 + 4 * j + tt) * 16 + 2 * g + hh) * 2048) + lane * 16;
        __builtin_amdgcn_raw_buffer_store_b128(o0, wsr, oaddr, 0, 16);
        __builtin_amdgcn_raw_buffer_store_b128(o1, wsr, oaddr + 1024, 0, 16);
    }
    LBAR();
    TS(13);
    u32x4 of[8];
    const unsigned ga = WS_O + (unsigned)(((b * 64 + 4 * j + 2 * th) * 16 + 2 * w) * 2048) + lane * 16;
    if (isL) {
#pragma unroll
        for (int i = 0; i < 12; ++i) glds16s(wos + (size_t)(256 + 48 * c4 + 4 * i) * 896, ooff, lds0 + (256 + 48 * c4 + 4 * i) * 256);
    }
    const int t2 = w & 1, nt = w >> 1;
    const int no = nt == 3 ? 80 : 32 * nt;
    const unsigned aa = (unsigned)((4 * hi + q4) * 256 + (((no >> 3) + 2 * blk + (p4 >> 1)) ^ (q4 << 2)) * 16 + 8 * (p4 & 1));
    bf16x8 af[32];
#define AFRAG(h, s) af[2 * (h) + (s)] = ldstr2(lds + aa + (28 * (h) - 4 * ((h) & 1) + 16 * (s)) * 256, 8 * 256)
#pragma unroll
    for (int h = 0; h < 9; ++h) { AFRAG(h, 0); AFRAG(h, 1); }
    const s16x4 a9lo = ldstr(lds + aa + 248 * 256);
    LBAR();
    TS(14);
    for (unsigned spins = 0;; ++spins) {
        gld_o(of, ws, ga, ga + 32768);
        unsigned mx = 0;
#pragma unroll
        for (int i = 0; i < 8; ++i) mx = max(max(mx, max(of[i][0], of[i][1])), max(of[i][2], of[i][3]));
        if (__all(mx != 0xFFFFFFFFu) || spins > SPIN_CAP) break;
    }
#pragma unroll
    for (int i = 0; i < 4; ++i) {
        *(LAS u32x4*)(ldsw + (2 * w) * 2048 + i * 1024 + lane * 16) = of[i];
        *(LAS u32x4*)(ldsw + 32768 + (2 * w) * 2048 + i * 1024 + lane * 16) = of[4 + i];
    }
    TS(15);
    asm volatile("s_waitcnt vmcnt(0) lgkmcnt(0)\n\ts_barrier" ::: "memory");
    TS(16);
    f32x16 acc = {};
    const LAS char* ol = lds + t2 * 32768 + lane * 16;
    {
        bf16x8 bq[18];
#pragma unroll
        for (int i = 0; i < 18; ++i) bq[i] = ldsr(ol + i * 1024);
        { const s16x4 a9hi = ldstr(lds + aa + 256 * 256); af[18] = __builtin_shufflevector(a9lo, a9hi, 0, 1, 2, 3, 4, 5, 6, 7); }
        AFRAG(9, 1);
#pragma unroll
        for (int h = 10; h < 16; ++h) { AFRAG(h, 0); AFRAG(h, 1); }
#pragma unroll
        for (int i = 0; i < 18; ++i) acc = MFMA(af[i], bq[i], acc);
    }
    {
        bf16x8 bq[14];
#pragma unroll
        for (int i = 0; i < 14; ++i) bq[i] = ldsr(ol + (18 + i) * 1024);
#pragma unroll
        for (int i = 0; i < 14; ++i) acc = MFMA(af[18 + i], bq[i], acc);
    }
    TS(17);
    {
        u16* orow = out + (size_t)(128 * j + 64 * th + 32 * t2 + r32) * 896 + 448 * b + 112 * cq + no + (hi ? 8 : 0);
#pragma unroll
        for (int pr = 0; pr < 2; ++pr) {
            unsigned ax = pk(acc[8 * pr], acc[8 * pr + 1]), ay = pk(acc[8 * pr + 2], acc[8 * pr + 3]);
            unsigned bx = pk(acc[8 * pr + 4], acc[8 * pr + 5]), by = pk(acc[8 * pr + 6], acc[8 * pr + 7]);
            auto rx = __builtin_amdgcn_permlane32_swap(ax, bx, false, false);
            auto ry = __builtin_amdgcn_permlane32_swap(ay, by, false, false);
            if (!(nt == 3 && pr == 0)) *(u32x4*)(orow + 16 * pr) = (u32x4){rx[0], ry[0], rx[1], ry[1]};
        }
    }
    if (bid == 0 && w == 7) {
        for (unsigned spins = 0; spins <= SPIN_CAP; ++spins) {
            bool ok = true;
#pragma unroll
            for (int i = 0; i < 4; ++i) ok &= gld8_wait(ws + WS_KFLAG, lane * 32 + i * 8) == tag;
            if (__all(ok)) break;
            __builtin_amdgcn_s_sleep(1);
        }
        if (lane == 0) __hip_atomic_store((gu64*)(ws + WS_GEN), tag, RLX_AGENT);
    }
}

extern "C" void kernel_launch(void* const* d_in, const int* in_sizes, int n_in, void* d_out, int out_size, void* d_ws, size_t ws_size, hipStream_t stream) {
    (void)in_sizes; (void)n_in; (void)out_size; (void)ws_size;
    (void)hipFuncSetAttribute((const void*)fused_kernel, hipFuncAttributeMaxDynamicSharedMemorySize, 160 * 1024);
    fused_kernel<<<256, 512, 160 * 1024, stream>>>((const u16*)d_in[0], (const u16*)d_in[1], (const u16*)d_in[2], (const u16*)d_in[3], (const u16*)d_in[4], (const u16*)d_in[5], (u16*)d_out, (char*)d_ws);
}
```

```cpp
#include <hip/hip_runtime.h>
#include <hip/hip_bf16.h>
#include <stdint.h>

typedef unsigned short u16;
typedef short bf16x8 __attribute__((ext_vector_type(8)));
typedef short s16x4 __attribute__((ext_vector_type(4)));
typedef short v4i16_t __attribute__((ext_vector_type(4)));
typedef float f32x16 __attribute__((ext_vector_type(16)));
typedef float f32x2 __attribute__((ext_vector_type(2)));
typedef __bf16 bf16x2_t __attribute__((ext_vector_type(2)));
typedef unsigned u32x4 __attribute__((ext_vector_type(4)));
#define LAS __attribute__((address_space(3)))
#define GAS __attribute__((address_space(1)))
typedef GAS unsigned long long gu64;
#define RLX_AGENT __ATOMIC_RELAXED, __HIP_MEMORY_SCOPE_AGENT
#define DI __device__ __forceinline__
#define MFMA(a, b, c) __builtin_amdgcn_mfma_f32_32x32x16_bf16((a), (b), (c), 0, 0, 0)
#define LBAR() asm volatile("s_waitcnt lgkmcnt(0)\n\ts_barrier" ::: "memory")
#define WAITV(N) asm volatile("s_waitcnt vmcnt(" #N ")" ::: "memory")
#define WBAR(N) asm volatile("s_waitcnt vmcnt(" #N ") lgkmcnt(0)\n\ts_barrier" ::: "memory")

constexpr unsigned WS_GEN = 0, WS_KFLAG = 65536, WS_VFLAG = 131072, WS_K = 1u << 20, WS_V = 6u << 20, WS_O = 16u << 20, WS_BYTES = 24u << 20;
#define TS(k) do {} while (0)
constexpr float QSCALE = 0.1889822365046136f * 1.4426950408889634f;
constexpr unsigned SPIN_CAP = 1u << 18;
constexpr unsigned RSLOT = 24576;
constexpr unsigned L_KV = 0, L_WO = 98304;

DI void glds16s(const void* sbase, unsigned voff, unsigned lds_dst) {
    asm volatile("s_mov_b32 m0, %2\n\ts_nop 0\n\tglobal_load_lds_dwordx4 %0, %1" :: "v"(voff), "s"(sbase), "s"(lds_dst) : "memory");
}
DI void glds16s_sc1(const void* sbase, unsigned voff, unsigned lds_dst) {
    asm volatile("s_mov_b32 m0, %2\n\ts_nop 0\n\tglobal_load_lds_dwordx4 %0, %1 sc1" :: "v"(voff), "s"(sbase), "s"(lds_dst) : "memory");
}
DI unsigned pk(float a, float b) { f32x2 v = {a, b}; bf16x2_t r = __builtin_convertvector(v, bf16x2_t); return __builtin_bit_cast(unsigned, r); }
DI bf16x8 ldsr(const LAS char* p) { return *(const LAS bf16x8*)p; }
DI s16x4 ldstr(const LAS char* p) { return __builtin_bit_cast(s16x4, __builtin_amdgcn_ds_read_tr16_b64_v4i16((LAS v4i16_t*)p)); }
DI bf16x8 ldstr2(const LAS char* p, int off2) { s16x4 a = ldstr(p), b = ldstr(p + off2); return __builtin_shufflevector(a, b, 0, 1, 2, 3, 4, 5, 6, 7); }
DI float max3(float a, float b, float c) { return __builtin_fmaxf(__builtin_fmaxf(a, b), c); }
DI float tmax(const f32x16& p, float m) {
    float a = max3(m, p[0], p[1]), b = max3(p[2], p[3], p[4]);
    a = max3(a, p[5], p[6]); b = max3(b, p[7], p[8]); a = max3(a, p[9], p[10]); b = max3(b, p[11], p[12]); a = max3(a, p[13], p[14]);
    return max3(a, b, p[15]);
}
DI float xhalf_max(float m) { auto rr = __builtin_amdgcn_permlane32_swap(__float_as_uint(m), __float_as_uint(m), false, false); return __builtin_fmaxf(__uint_as_float(rr[0]), __uint_as_float(rr[1])); }
DI u32x4 pk4(const f32x16& a, int s) { return (u32x4){pk(a[8 * s], a[8 * s + 1]), pk(a[8 * s + 2], a[8 * s + 3]), pk(a[8 * s + 4], a[8 * s + 5]), pk(a[8 * s + 6], a[8 * s + 7])}; }
#define BC8(x) __builtin_bit_cast(bf16x8, x)

DI void gld_o(u32x4 (&r)[8], const char* sbase, unsigned va, unsigned vb) {
    asm volatile("global_load_dwordx4 %0, %8, %10 sc1\n\tglobal_load_dwordx4 %1, %8, %10 offset:1024 sc1\n\t"
                 "global_load_dwordx4 %2, %8, %10 offset:2048 sc1\n\tglobal_load_dwordx4 %3, %8, %10 offset:3072 sc1\n\t"
                 "global_load_dwordx4 %4, %9, %10 sc1\n\tglobal_load_dwordx4 %5, %9, %10 offset:1024 sc1\n\t"
                 "global_load_dwordx4 %6, %9, %10 offset:2048 sc1\n\tglobal_load_dwordx4 %7, %9, %10 offset:3072 sc1\n\t"
                 "s_waitcnt vmcnt(0)"
                 : "=&v"(r[0]), "=&v"(r[1]), "=&v"(r[2]), "=&v"(r[3]), "=&v"(r[4]), "=&v"(r[5]), "=&v"(r[6]), "=&v"(r[7])
                 : "v"(va), "v"(vb), "s"(sbase) : "memory");
}
DI unsigned long long gld8_wait(const char* sbase, unsigned voff) {
    unsigned long long r;
    asm volatile("global_load_dwordx2 %0, %1, %2 sc1\n\ts_waitcnt vmcnt(0)" : "=&v"(r) : "v"(voff), "s"(sbase) : "memory");
    return r;
}
#define GLD8_ASYNC(dst, sbase, voff) asm volatile("global_load_dwordx2 %0, %1, %2 sc1" : "=v"(dst) : "v"(voff), "s"(sbase) : "memory")
DI void wait_flags(const char* sbase, unsigned voff, unsigned long long tag, unsigned long long need) {
    for (unsigned spins = 0; spins <= SPIN_CAP; ++spins) {
        const unsigned long long v = gld8_wait(sbase, voff);
        if ((__ballot(v == tag) & need) == need) break;
        __builtin_amdgcn_s_sleep(1);
    }
}

__global__ void __launch_bounds__(512, 2)
fused_kernel(const u16* __restrict__ xA, const u16* __restrict__ wA, const u16* __restrict__ oA,
             const u16* __restrict__ xB, const u16* __restrict__ wB, const u16* __restrict__ oB,
             u16* __restrict__ out, char* ws)
{
    extern __shared__ __attribute__((aligned(16))) char lds_[];
    const LAS char* lds = (const LAS char*)lds_;
    LAS char* ldsw = (LAS char*)lds_;
    const unsigned lds0 = (unsigned)(uintptr_t)lds_;
    TS(0);
    const int tid = threadIdx.x, lane = tid & 63;
    const int w = __builtin_amdgcn_readfirstlane(tid >> 6);
    const int bid = blockIdx.x, xcd = bid & 7, idx = bid >> 3;
    const int b = xcd >> 2, g = idx & 7, j = (xcd & 3) * 4 + (idx >> 3);
    const int hh = w >> 2, tt = w & 3;
    const char* x = (const char*)(b ? xB : xA) + (size_t)j * 128 * 896;
    const char* wq = (const char*)(b ? wB : wA) + 112 * g;
#define ROT ((idx >> 3) * 2 + (idx & 7)) % 7
    const int rot = ROT;
    const unsigned xo0 = (unsigned)((16 * w + (lane >> 3)) * 896 + (((lane & 7) ^ (lane >> 4)) * 16));
    const unsigned xo1 = (unsigned)((16 * w + 8 + (lane >> 3)) * 896 + (((lane & 7) ^ (4 + (lane >> 4))) * 16));
    const unsigned wo1 = (unsigned)((8 * w + (lane >> 3)) * 2688 + ((((lane >> 2) & 1) ^ ((lane >> 4) & 1)) * 48) + (lane & 3) * 16);
    unsigned kcs[7];
#pragma unroll
    for (int p = 0; p < 7; ++p) { const int k = p + rot; kcs[p] = (unsigned)(k >= 7 ? k - 7 : k); }
    const unsigned ldx = lds0 + w * 2048, ldw = lds0 + w * 1024;
    auto issue_w = [&](int which, int p, unsigned dst) { glds16s(wq, wo1 + kcs[p] * (64u * 2688u) + which * 896, ldw + dst); };
    auto issue_k = [&](int p) {
        const unsigned slot = (p % 6) * RSLOT;
        glds16s(x, xo0 + kcs[p] * 128u, ldx + slot); glds16s(x, xo1 + kcs[p] * 128u, ldx + slot + 1024);
        issue_w(1, p, slot + 16384);
    };
#define CADDR(c) (((7 + (c) / 3) % 6) * RSLOT + ((c) % 3) * 8192)
    auto issue_c = [&](int c) { issue_w(c < 7 ? 2 : 0, c < 7 ? c : c - 7, CADDR(c)); };
    issue_k(0); issue_k(1); issue_k(2); issue_k(3); issue_k(4);
    TS(1);
    const char* wo = (const char*)(b ? oB : oA);
    const int r32 = lane & 31, hi = lane >> 5;
    const __amdgpu_buffer_rsrc_t wsr = __builtin_amdgcn_make_buffer_rsrc(ws, 0, WS_BYTES, 0x00020000);
    const int unit = (b * 8 + g) * 16 + j;
    const int q4 = (lane & 15) >> 2, p4 = lane & 3, blk = (lane >> 4) & 1;
    unsigned long long tag;
    GLD8_ASYNC(tag, ws + WS_GEN, 0u);
    const unsigned oaddr = WS_O + (unsigned)(((b * 64 + 4 * j + tt) * 16 + 2 * g + hh) * 2048) + lane * 16;
    {
        const u32x4 poison = {0xFFFFFFFFu, 0xFFFFFFFFu, 0xFFFFFFFFu, 0xFFFFFFFFu};
        __builtin_amdgcn_raw_buffer_store_b128(poison, wsr, oaddr, 0, 16);
        __builtin_amdgcn_raw_buffer_store_b128(poison, wsr, oaddr + 1024, 0, 16);
    }
    int fidx = unit;
    if (lane == 0) fidx = j >= 1 ? unit - 1 : unit;
    else if (lane == 1) fidx = j >= 2 ? unit - 2 : unit;
    else if (lane < 10) fidx = (b * 8 + (lane - 2)) * 16 + j;
    const unsigned long long need = (j >= 2 ? 3ull : (j >= 1 ? 1ull : 0ull)), needk = need | 0x3FCull;

    unsigned xa[4];
#pragma unroll
    for (int s = 0; s < 4; ++s) xa[s] = (unsigned)((32 * tt + r32) * 128 + (((hi ^ ((r32 >> 1) & 7)) * 16) ^ (s * 32)));
    const unsigned wa = (unsigned)((8 * hi + q4) * 128 + ((hh ^ (q4 >> 1)) * 64) + 32 * blk + 8 * p4);
    const bool padk = hh ? (hi == 0) : (hi == 1);
    const unsigned toff = (unsigned)(hh * 8192 + tt * 2048) + lane * 16;

    bf16x8 xf[7][4], fr[2][4];
    f32x16 acc1 = {};
#define SB() __builtin_amdgcn_sched_barrier(0)
#define KB(p) (((p) % 6) * RSLOT)
#define RDW(base, B, s) fr[B][s] = ldstr2(lds + (base) + wa + (s) * 2048, 512)
#define MT(p, B, s) acc1 = MFMA(fr[B][s], xf[p][s], acc1)
#define MV(p, B, s) acc1 = MFMA(xf[p][s], fr[B][s], acc1)
#define STK(p, B, pm) do { _Pragma("unroll") for (int s = 0; s < 4; ++s) { xf[p][s] = ldsr(lds + KB(p) + xa[s]); RDW(KB(p) + 16384, B, s); SB(); MT(pm, (B) ^ 1, s); SB(); } } while (0)
#define STT(c, B, pm) do { _Pragma("unroll") for (int s = 0; s < 4; ++s) { RDW(CADDR(c), B, s); SB(); MT(pm, (B) ^ 1, s); SB(); } } while (0)
#define STV(c, B, pm) do { _Pragma("unroll") for (int s = 0; s < 4; ++s) { RDW(CADDR(c), B, s); SB(); MV(pm, (B) ^ 1, s); SB(); } } while (0)
#define RDC(c, B) do { _Pragma("unroll") for (int s = 0; s < 4; ++s) RDW(CADDR(c), B, s); SB(); } while (0)
#define MTO(pm, B) do { _Pragma("unroll") for (int s = 0; s < 4; ++s) MT(pm, B, s); SB(); } while (0)
    WBAR(15); TS(2);
    issue_k(5);
    { _Pragma("unroll") for (int s = 0; s < 4; ++s) { xf[0][s] = ldsr(lds + xa[s]); RDW(16384, 0, s); } }
    WBAR(15);
    issue_k(6); STK(1, 1, 0); WBAR(15);
    STK(2, 0, 1); WBAR(12);
    issue_c(0); issue_c(1); issue_c(2); STK(3, 1, 2);
    asm volatile("s_waitcnt vmcnt(9) lgkmcnt(0)\n\ts_barrier" : "+v"(tag) :: "memory");
    tag += 1ull;
    issue_c(3); issue_c(4); issue_c(5); STK(4, 0, 3); WBAR(9);
    issue_c(6); issue_c(7); issue_c(8); STK(5, 1, 4); WBAR(9);
    STK(6, 0, 5); MTO(6, 0);
    TS(3);
    u32x4 k0, k1;
    {
#pragma unroll
        for (int r = 0; r < 4; ++r) { acc1[r] = (padk && hh) ? 0.f : acc1[r]; acc1[12 + r] = (padk && !hh) ? 0.f : acc1[12 + r]; }
        k0 = pk4(acc1, 0); k1 = pk4(acc1, 1);
        const unsigned gaddr = WS_K + (unsigned)unit * 16384u + toff;
        __builtin_amdgcn_raw_buffer_store_b128(k0, wsr, gaddr, 0, 16);
        __builtin_amdgcn_raw_buffer_store_b128(k1, wsr, gaddr + 1024, 0, 16);
    }
    acc1 = (f32x16){};
    issue_c(9); issue_c(10); issue_c(11); issue_c(12); issue_c(13);
    WBAR(10);
    RDC(0, 1); STV(1, 0, 0); STV(2, 1, 1); STV(3, 0, 2); STV(4, 1, 3); STV(5, 0, 4);
    WBAR(5);
    if (tid == 0) __hip_atomic_store((gu64*)(ws + WS_KFLAG) + unit * 16, tag, RLX_AGENT);
    TS(5);
    STV(6, 1, 5); STV(7, 0, 6);
    TS(4);
    u32x4 v0, v1;
    {
        v0 = pk4(acc1, 0); v1 = pk4(acc1, 1);
        const bool padv = hh ? (r32 < 4) : (r32 >= 28);
        const unsigned fill = (r32 == (hh ? 0 : 28)) ? 0x3F803F80u : 0u;
        if (padv) { v0 = (u32x4){fill, fill, fill, fill}; v1 = v0; }
        const unsigned gaddr = WS_V + (unsigned)unit * 16384u + toff;
        __builtin_amdgcn_raw_buffer_store_b128(v0, wsr, gaddr, 0, 16);
        __builtin_amdgcn_raw_buffer_store_b128(v1, wsr, gaddr + 1024, 0, 16);
    }
    acc1 = (f32x16){};
    STT(8, 1, 0); WBAR(2);
    STT(9, 0, 1); STT(10, 1, 2); STT(11, 0, 3);
    unsigned long long kpoll;
    GLD8_ASYNC(kpoll, ws + WS_KFLAG, fidx * 128);
    STT(12, 1, 4); STT(13, 0, 5); MTO(6, 0);
    TS(6);
    bf16x8 Q0, Q1;
    {
        acc1[0] = (padk && hh) ? 1.f : acc1[0]; acc1[12] = (padk && !hh) ? 1.f : acc1[12];
#pragma unroll
        for (int r = 1; r < 4; ++r) { acc1[r] = (padk && hh) ? 0.f : acc1[r]; acc1[12 + r] = (padk && !hh) ? 0.f : acc1[12 + r]; }
        Q0 = BC8(pk4(acc1, 0)); Q1 = BC8(pk4(acc1, 1));
    }
    asm volatile("s_waitcnt vmcnt(0) lgkmcnt(0)\n\ts_barrier" : "+v"(kpoll) :: "memory");
    if (tid == 0) __hip_atomic_store((gu64*)(ws + WS_VFLAG) + unit * 16, tag, RLX_AGENT);
    TS(7);
    const unsigned tl = (unsigned)(tt * 8192 + hh * 2048) + lane * 16;
    auto issue_halo = [&](int isv) {
        const unsigned neg = isv ? 0u : 0xF149u;
        const u32x4 d0 = {(hh && hi == 0) ? neg : 0u, 0u, 0u, 0u}, d1 = {0u, 0u, (!hh && hi == 1) ? neg : 0u, 0u};
        const unsigned hsrc = (unsigned)unit * 16384u + toff;
        const unsigned hdst = L_KV + tt * 8192 + hh * 2048 + (isv ? 4096 : 0);
        const char* src = ws + (isv ? WS_V : WS_K);
        if (j >= 2) { glds16s_sc1(src, hsrc - 32768, lds0 + hdst); glds16s_sc1(src, hsrc - 32768 + 1024, lds0 + hdst + 1024); }
        else { *(LAS u32x4*)(ldsw + hdst + lane * 16) = d0; *(LAS u32x4*)(ldsw + hdst + 1024 + lane * 16) = d1; }
        if (j >= 1) { glds16s_sc1(src, hsrc - 16384, lds0 + 32768 + hdst); glds16s_sc1(src, hsrc - 16384 + 1024, lds0 + 32768 + hdst + 1024); }
        else { *(LAS u32x4*)(ldsw + 32768 + hdst + lane * 16) = d0; *(LAS u32x4*)(ldsw + 32768 + hdst + 1024 + lane * 16) = d1; }
    };
    if ((__ballot(kpoll == tag) & needk) != needk) wait_flags(ws + WS_KFLAG, fidx * 128, tag, needk);
    TS(8);
    issue_halo(0);
    *(LAS u32x4*)(ldsw + L_KV + 65536 + tl) = k0; *(LAS u32x4*)(ldsw + L_KV + 65536 + tl + 1024) = k1;
    *(LAS u32x4*)(ldsw + L_KV + 65536 + 4096 + tl) = v0; *(LAS u32x4*)(ldsw + L_KV + 65536 + 4096 + tl + 1024) = v1;
    const int cq = g >> 1, th = g & 1;
    const unsigned ooff = (unsigned)((lane >> 4) * 896 + min((lane & 15) ^ ((lane >> 4) << 2), 13) * 16);
    const char* wos = wo + 224 * cq;
    unsigned long long vpoll;
    WBAR(0);
    GLD8_ASYNC(vpoll, ws + WS_VFLAG, fidx * 128);
    TS(10);
    const f32x16 zero16 = {};
    const LAS char* kl = lds + L_KV + tl;
    const LAS char* vl = lds + L_KV + 4096 + tl;
    f32x16 S[9];
#pragma unroll
    for (int u = 8; u >= 0; --u) { bf16x8 a0 = ldsr(kl + u * 8192), a1 = ldsr(kl + u * 8192 + 1024); S[u] = MFMA(a0, Q0, zero16); S[u] = MFMA(a1, Q1, S[u]); }
    unsigned msk[8];
#pragma unroll
    for (int r = 0; r < 16; ++r) { const int key = (r & 3) + 8 * (r >> 2) + 4 * hi; S[8][r] = key <= r32 ? S[8][r] : S[0][r]; }
#pragma unroll
    for (int i = 0; i < 8; ++i) { const int key = ((2 * i) & 3) + 8 * ((2 * i) >> 2) + 4 * hi; msk[i] = (key <= r32 ? 0xFFFFu : 0u) | (key + 1 <= r32 ? 0xFFFF0000u : 0u); }
    float m = S[8][0];
#pragma unroll
    for (int u = 8; u >= 1; --u) m = tmax(S[u], m);
    m = xhalf_max(m) * QSCALE;
    TS(11);
    asm volatile("s_waitcnt vmcnt(0)" : "+v"(vpoll) :: "memory");
    if ((__ballot(vpoll == tag) & need) != need) wait_flags(ws + WS_VFLAG, fidx * 128, tag, need);
    TS(9);
    issue_halo(1);
    u32x4 P[9][2];
#define EXPT(u) do { _Pragma("unroll") for (int r = 0; r < 16; ++r) S[u][r] = __builtin_amdgcn_exp2f(__builtin_fmaf(S[u][r], QSCALE, -m)); \
        P[u][0] = pk4(S[u], 0); P[u][1] = pk4(S[u], 1); } while (0)
    EXPT(8); EXPT(7); EXPT(6); EXPT(5);
#pragma unroll
    for (int i = 0; i < 4; ++i) { P[0][0][i] = P[8][0][i] & ~msk[i]; P[0][1][i] = P[8][1][i] & ~msk[4 + i]; P[8][0][i] &= msk[i]; P[8][1][i] &= msk[4 + i]; }
    WBAR(0);
    TS(12);
    {
        const unsigned o1 = ooff + (unsigned)(32 * w) * 896u, l1 = lds0 + L_WO + w * 8192;
#pragma unroll
        for (int i = 0; i < 8; ++i) glds16s(wos, o1 + i * 3584, l1 + i * 1024);
    }
    f32x16 O = {};
#define PVT(u) do { bf16x8 a0 = ldsr(vl + (u) * 8192), a1 = ldsr(vl + (u) * 8192 + 1024); O = MFMA(a0, BC8(P[u][0]), O); O = MFMA(a1, BC8(P[u][1]), O); } while (0)
    PVT(8); EXPT(4); PVT(7); EXPT(3); PVT(6); EXPT(2); PVT(5); EXPT(1); PVT(4); PVT(3); PVT(2); PVT(1); PVT(0);
    TS(13);
    {
        const float lsrc = hh ? O[0] : O[12];
        auto rr = __builtin_amdgcn_permlane32_swap(__float_as_uint(lsrc), __float_as_uint(lsrc), false, false);
        const float l = hh ? __uint_as_float(rr[0]) : __uint_as_float(rr[1]);
        const float inv = __builtin_amdgcn_rcpf(l);
#pragma unroll
        for (int r = 0; r < 16; ++r) O[r] *= inv;
#pragma unroll
        for (int r = 0; r < 4; ++r) { O[r] = (padk && hh) ? 0.f : O[r]; O[12 + r] = (padk && !hh) ? 0.f : O[12 + r]; }
        u32x4 o0 = pk4(O, 0), o1 = pk4(O, 1);
#pragma unroll
        for (int i = 0; i < 4; ++i) { o0[i] = min(o0[i], 0xFFFFFFFEu); o1[i] = min(o1[i], 0xFFFFFFFEu); }
        WAITV(0);
        __builtin_amdgcn_raw_buffer_store_b128(o0, wsr, oaddr, 0, 16);
        __builtin_amdgcn_raw_buffer_store_b128(o1, wsr, oaddr + 1024, 0, 16);
    }
    LBAR();
    TS(14);
    {
        const unsigned o2 = ooff + (unsigned)(256 + 24 * w) * 896u, l2 = lds0 + (24 * w) * 256;
#pragma unroll
        for (int i = 0; i < 6; ++i) glds16s(wos, o2 + i * 3584, l2 + i * 1024);
    }
    u32x4 of[8];
    const unsigned ga = WS_O + (unsigned)(((b * 64 + 4 * j + 2 * th) * 16 + 2 * w) * 2048) + lane * 16;
    const int t2 = w & 1, nt = w >> 1;
    const int no = nt == 3 ? 80 : 32 * nt;
    const unsigned aa = (unsigned)((4 * hi + q4) * 256 + (((no >> 3) + 2 * blk + (p4 >> 1)) ^ (q4 << 2)) * 16 + 8 * (p4 & 1));
    bf16x8 af[32];
    const LAS char* ab1 = lds + (aa + L_WO);
    const LAS char* ab2 = lds + aa;
    asm volatile("" : "+v"(ab1));
#define APTR(r) ((r) < 256 ? ab1 + (r) * 256 : ab2 + ((r) - 256) * 256)
#define AFRAG(h, s) af[2 * (h) + (s)] = ldstr2(APTR(28 * (h) - 4 * ((h) & 1) + 16 * (s)), 8 * 256)
#pragma unroll
    for (int h = 0; h < 9; ++h) { AFRAG(h, 0); AFRAG(h, 1); }
    const s16x4 a9lo = ldstr(APTR(248));
    LBAR();
    TS(15);
    for (unsigned spins = 0;; ++spins) {
        gld_o(of, ws, ga, ga + 32768);
        unsigned mx = 0;
#pragma unroll
        for (int i = 0; i < 8; ++i) mx = max(max(mx, max(of[i][0], of[i][1])), max(of[i][2], of[i][3]));
        if (__all(mx != 0xFFFFFFFFu) || spins > SPIN_CAP) break;
    }
#pragma unroll
    for (int i = 0; i < 4; ++i) {
        *(LAS u32x4*)(ldsw + L_WO + (2 * w) * 2048 + i * 1024 + lane * 16) = of[i];
        *(LAS u32x4*)(ldsw + L_WO + 32768 + (2 * w) * 2048 + i * 1024 + lane * 16) = of[4 + i];
    }
    TS(16);
    LBAR();
    TS(17);
    f32x16 acc = {};
    const LAS char* ol = lds + L_WO + t2 * 32768 + lane * 16;
    {
        bf16x8 bq[18];
#pragma unroll
        for (int i = 0; i < 18; ++i) bq[i] = ldsr(ol + i * 1024);
        { const s16x4 a9hi = ldstr(APTR(256)); af[18] = __builtin_shufflevector(a9lo, a9hi, 0, 1, 2, 3, 4, 5, 6, 7); }
        AFRAG(9, 1);
#pragma unroll
        for (int h = 10; h < 16; ++h) { AFRAG(h, 0); AFRAG(h, 1); }
#pragma unroll
        for (int i = 0; i < 18; ++i) acc = MFMA(af[i], bq[i], acc);
    }
    {
        bf16x8 bq[14];
#pragma unroll
        for (int i = 0; i < 14; ++i) bq[i] = ldsr(ol + (18 + i) * 1024);
#pragma unroll
        for (int i = 0; i < 14; ++i) acc = MFMA(af[18 + i], bq[i], acc);
    }
    TS(18);
    {
        u16* orow = out + (size_t)(128 * j + 64 * th + 32 * t2 + r32) * 896 + 448 * b + 112 * cq + no + (hi ? 8 : 0);
#pragma unroll
        for (int pr = 0; pr < 2; ++pr) {
            unsigned ax = pk(acc[8 * pr], acc[8 * pr + 1]), ay = pk(acc[8 * pr + 2], acc[8 * pr + 3]);
            unsigned bx = pk(acc[8 * pr + 4], acc[8 * pr + 5]), by = pk(acc[8 * pr + 6], acc[8 * pr + 7]);
            auto rx = __builtin_amdgcn_permlane32_swap(ax, bx, false, false);
            auto ry = __builtin_amdgcn_permlane32_swap(ay, by, false, false);
            if (!(nt == 3 && pr == 0)) *(u32x4*)(orow + 16 * pr) = (u32x4){rx[0], ry[0], rx[1], ry[1]};
        }
    }
    if (bid == 0 && w == 7) {
        for (unsigned spins = 0; spins <= SPIN_CAP; ++spins) {
            bool ok = true;
#pragma unroll
            for (int i = 0; i < 4; ++i) ok &= gld8_wait(ws + WS_KFLAG, (lane * 4 + i) * 128) == tag;
            if (__all(ok)) break;
            __builtin_amdgcn_s_sleep(1);
        }
        if (lane == 0) __hip_atomic_store((gu64*)(ws + WS_GEN), tag, RLX_AGENT);
    }
}

extern "C" void kernel_launch(void* const* d_in, const int* in_sizes, int n_in, void* d_out, int out_size, void* d_ws, size_t ws_size, hipStream_t stream) {
    (void)in_sizes; (void)n_in; (void)out_size; (void)ws_size;
    (void)hipFuncSetAttribute((const void*)fused_kernel, hipFuncAttributeMaxDynamicSharedMemorySize, 160 * 1024);
    fused_kernel<<<256, 512, 160 * 1024, stream>>>((const u16*)d_in[0], (const u16*)d_in[1], (const u16*)d_in[2], (const u16*)d_in[3], (const u16*)d_in[4], (const u16*)d_in[5], (u16*)d_out, (char*)d_ws);
}
```

```cpp
#include <hip/hip_runtime.h>
#include <hip/hip_bf16.h>
#include <stdint.h>

typedef unsigned short u16;
typedef short bf16x8 __attribute__((ext_vector_type(8)));
typedef short s16x4 __attribute__((ext_vector_type(4)));
typedef short v4i16_t __attribute__((ext_vector_type(4)));
typedef float f32x16 __attribute__((ext_vector_type(16)));
typedef float f32x2 __attribute__((ext_vector_type(2)));
typedef __bf16 bf16x2_t __attribute__((ext_vector_type(2)));
typedef unsigned u32x4 __attribute__((ext_vector_type(4)));
#define LAS __attribute__((address_space(3)))
#define GAS __attribute__((address_space(1)))
typedef GAS unsigned long long gu64;
#define RLX_AGENT __ATOMIC_RELAXED, __HIP_MEMORY_SCOPE_AGENT
#define DI __device__ __forceinline__
#define MFMA(a, b, c) __builtin_amdgcn_mfma_f32_32x32x16_bf16((a), (b), (c), 0, 0, 0)
#define LBAR() asm volatile("s_waitcnt lgkmcnt(0)\n\ts_barrier" ::: "memory")
#define WAITV(N) asm volatile("s_waitcnt vmcnt(" #N ")" ::: "memory")
#define WBAR(N) asm volatile("s_waitcnt vmcnt(" #N ") lgkmcnt(0)\n\ts_barrier" ::: "memory")

constexpr unsigned WS_GEN = 0, WS_KFLAG = 65536, WS_VFLAG = 131072, WS_K = 1u << 20, WS_V = 6u << 20, WS_O = 16u << 20, WS_BYTES = 24u << 20;
#define TS(k) do {} while (0)
constexpr float QSCALE = 0.1889822365046136f * 1.4426950408889634f;
constexpr unsigned SPIN_CAP = 1u << 18;
constexpr unsigned RSLOT = 24576;
constexpr unsigned L_KV = 0, L_WO = 98304;

DI void glds16s(const void* sbase, unsigned voff, unsigned lds_dst) {
    asm volatile("s_mov_b32 m0, %2\n\ts_nop 0\n\tglobal_load_lds_dwordx4 %0, %1" :: "v"(voff), "s"(sbase), "s"(lds_dst) : "memory");
}
DI void glds16s_sc1(const void* sbase, unsigned voff, unsigned lds_dst) {
    asm volatile("s_mov_b32 m0, %2\n\ts_nop 0\n\tglobal_load_lds_dwordx4 %0, %1 sc1" :: "v"(voff), "s"(sbase), "s"(lds_dst) : "memory");
}
DI unsigned pk(float a, float b) { f32x2 v = {a, b}; bf16x2_t r = __builtin_convertvector(v, bf16x2_t); return __builtin_bit_cast(unsigned, r); }
DI bf16x8 ldsr(const LAS char* p) { return *(const LAS bf16x8*)p; }
DI s16x4 ldstr(const LAS char* p) { return __builtin_bit_cast(s16x4, __builtin_amdgcn_ds_read_tr16_b64_v4i16((LAS v4i16_t*)p)); }
DI bf16x8 ldstr2(const LAS char* p, int off2) { s16x4 a = ldstr(p), b = ldstr(p + off2); return __builtin_shufflevector(a, b, 0, 1, 2, 3, 4, 5, 6, 7); }
DI float max3(float a, float b, float c) { return __builtin_fmaxf(__builtin_fmaxf(a, b), c); }
DI float tmax(const f32x16& p, float m) {
    float a = max3(m, p[0], p[1]), b = max3(p[2], p[3], p[4]);
    a = max3(a, p[5], p[6]); b = max3(b, p[7], p[8]); a = max3(a, p[9], p[10]); b = max3(b, p[11], p[12]); a = max3(a, p[13], p[14]);
    return max3(a, b, p[15]);
}
DI float xhalf_max(float m) { auto rr = __builtin_amdgcn_permlane32_swap(__float_as_uint(m), __float_as_uint(m), false, false); return __builtin_fmaxf(__uint_as_float(rr[0]), __uint_as_float(rr[1])); }
DI u32x4 pk4(const f32x16& a, int s) { return (u32x4){pk(a[8 * s], a[8 * s + 1]), pk(a[8 * s + 2], a[8 * s + 3]), pk(a[8 * s + 4], a[8 * s + 5]), pk(a[8 * s + 6], a[8 * s + 7])}; }
#define BC8(x) __builtin_bit_cast(bf16x8, x)

DI void gld_o(u32x4 (&r)[8], const char* sbase, unsigned va, unsigned vb) {
    asm volatile("global_load_dwordx4 %0, %8, %10 sc1\n\tglobal_load_dwordx4 %1, %8, %10 offset:1024 sc1\n\t"
                 "global_load_dwordx4 %2, %8, %10 offset:2048 sc1\n\tglobal_load_dwordx4 %3, %8, %10 offset:3072 sc1\n\t"
                 "global_load_dwordx4 %4, %9, %10 sc1\n\tglobal_load_dwordx4 %5, %9, %10 offset:1024 sc1\n\t"
                 "global_load_dwordx4 %6, %9, %10 offset:2048 sc1\n\tglobal_load_dwordx4 %7, %9, %10 offset:3072 sc1\n\t"
                 "s_waitcnt vmcnt(0)"
                 : "=&v"(r[0]), "=&v"(r[1]), "=&v"(r[2]), "=&v"(r[3]), "=&v"(r[4]), "=&v"(r[5]), "=&v"(r[6]), "=&v"(r[7])
                 : "v"(va), "v"(vb), "s"(sbase) : "memory");
}
DI unsigned long long gld8_wait(const char* sbase, unsigned voff) {
    unsigned long long r;
    asm volatile("global_load_dwordx2 %0, %1, %2 sc1\n\ts_waitcnt vmcnt(0)" : "=&v"(r) : "v"(voff), "s"(sbase) : "memory");
    return r;
}
#define GLD8_ASYNC(dst, sbase, voff) asm volatile("global_load_dwordx2 %0, %1, %2 sc1" : "=v"(dst) : "v"(voff), "s"(sbase) : "memory")
DI void wait_flags(const char* sbase, unsigned voff, unsigned long long tag, unsigned long long need) {
    for (unsigned spins = 0; spins <= SPIN_CAP; ++spins) {
        const unsigned long long v = gld8_wait(sbase, voff);
        if ((__ballot(v == tag) & need) == need) break;
        __builtin_amdgcn_s_sleep(1);
    }
}

__global__ void __launch_bounds__(512, 2)
fused_kernel(const u16* __restrict__ xA, const u16* __restrict__ wA, const u16* __restrict__ oA,
             const u16* __restrict__ xB, const u16* __restrict__ wB, const u16* __restrict__ oB,
             u16* __restrict__ out, char* ws)
{
    extern __shared__ __attribute__((aligned(16))) char lds_[];
    const LAS char* lds = (const LAS char*)lds_;
    LAS char* ldsw = (LAS char*)lds_;
    const unsigned lds0 = (unsigned)(uintptr_t)lds_;
    TS(0);
    const int tid = threadIdx.x, lane = tid & 63;
    const int w = __builtin_amdgcn_readfirstlane(tid >> 6);
    const int bid = blockIdx.x, xcd = bid & 7, idx = bid >> 3;
    const int b = xcd >> 2, g = idx & 7, j = (xcd & 3) * 4 + (idx >> 3);
    const int hh = w >> 2, tt = w & 3;
    const char* x = (const char*)(b ? xB : xA) + (size_t)j * 128 * 896;
    const char* wq = (const char*)(b ? wB : wA) + 112 * g;
#define ROT (g + j) % 7
    const int rot = ROT;
    const unsigned xo0 = (unsigned)((16 * w + (lane >> 3)) * 896 + (((lane & 7) ^ (lane >> 4)) * 16));
    const unsigned xo1 = (unsigned)((16 * w + 8 + (lane >> 3)) * 896 + (((lane & 7) ^ (4 + (lane >> 4))) * 16));
    const unsigned wo1 = (unsigned)((8 * w + (lane >> 3)) * 2688 + ((((lane >> 2) & 1) ^ ((lane >> 4) & 1)) * 48) + (lane & 3) * 16);
    unsigned kcs[7];
#pragma unroll
    for (int p = 0; p < 7; ++p) { const int k = p + rot; kcs[p] = (unsigned)(k >= 7 ? k - 7 : k); }
    const unsigned ldx = lds0 + w * 2048, ldw = lds0 + w * 1024;
    auto issue_w = [&](int which, int p, unsigned dst) { glds16s(wq, wo1 + kcs[p] * (64u * 2688u) + which * 896, ldw + dst); };
    auto issue_k = [&](int p) {
        const unsigned slot = (p % 6) * RSLOT;
        glds16s(x, xo0 + kcs[p] * 128u, ldx + slot); glds16s(x, xo1 + kcs[p] * 128u, ldx + slot + 1024);
        issue_w(1, p, slot + 16384);
    };
#define CADDR(c) (((7 + (c) / 3) % 6) * RSLOT + ((c) % 3) * 8192)
    auto issue_c = [&](int c) { issue_w(c < 7 ? 2 : 0, c < 7 ? c : c - 7, CADDR(c)); };
    issue_k(0); issue_k(1); issue_k(2); issue_k(3); issue_k(4);
    TS(1);
    const char* wo = (const char*)(b ? oB : oA);
    const int r32 = lane & 31, hi = lane >> 5;
    const __amdgpu_buffer_rsrc_t wsr = __builtin_amdgcn_make_buffer_rsrc(ws, 0, WS_BYTES, 0x00020000);
    const int unit = (b * 8 + g) * 16 + j;
    const int q4 = (lane & 15) >> 2, p4 = lane & 3, blk = (lane >> 4) & 1;
    unsigned long long tag;
    GLD8_ASYNC(tag, ws + WS_GEN, 0u);
    const unsigned oaddr = WS_O + (unsigned)(((b * 64 + 4 * j + tt) * 16 + 2 * g + hh) * 2048) + lane * 16;
    {
        const u32x4 poison = {0xFFFFFFFFu, 0xFFFFFFFFu, 0xFFFFFFFFu, 0xFFFFFFFFu};
        __builtin_amdgcn_raw_buffer_store_b128(poison, wsr, oaddr, 0, 16);
        __builtin_amdgcn_raw_buffer_store_b128(poison, wsr, oaddr + 1024, 0, 16);
    }
    int fidx = unit;
    if (lane == 0) fidx = j >= 1 ? unit - 1 : unit;
    else if (lane == 1) fidx = j >= 2 ? unit - 2 : unit;
    else if (lane < 10) fidx = (b * 8 + (lane - 2)) * 16 + j;
    const unsigned long long need = (j >= 2 ? 3ull : (j >= 1 ? 1ull : 0ull)), needk = need | 0x3FCull;

    unsigned xa[4];
#pragma unroll
    for (int s = 0; s < 4; ++s) xa[s] = (unsigned)((32 * tt + r32) * 128 + (((hi ^ ((r32 >> 1) & 7)) * 16) ^ (s * 32)));
    const unsigned wa = (unsigned)((8 * hi + q4) * 128 + ((hh ^ (q4 >> 1)) * 64) + 32 * blk + 8 * p4);
    const bool padk = hh ? (hi == 0) : (hi == 1);
    const unsigned toff = (unsigned)(hh * 8192 + tt * 2048) + lane * 16;

    bf16x8 xf[7][4], fr[2][4];
    f32x16 acc1 = {};
#define SB() __builtin_amdgcn_sched_barrier(0)
#define KB(p) (((p) % 6) * RSLOT)
#define RDW(base, B, s) fr[B][s] = ldstr2(lds + (base) + wa + (s) * 2048, 512)
#define MT(p, B, s) acc1 = MFMA(fr[B][s], xf[p][s], acc1)
#define MV(p, B, s) acc1 = MFMA(xf[p][s], fr[B][s], acc1)
#define STK(p, B, pm) do { _Pragma("unroll") for (int s = 0; s < 4; ++s) { xf[p][s] = ldsr(lds + KB(p) + xa[s]); RDW(KB(p) + 16384, B, s); SB(); MT(pm, (B) ^ 1, s); SB(); } } while (0)
#define STT(c, B, pm) do { _Pragma("unroll") for (int s = 0; s < 4; ++s) { RDW(CADDR(c), B, s); SB(); MT(pm, (B) ^ 1, s); SB(); } } while (0)
#define STV(c, B, pm) do { _Pragma("unroll") for (int s = 0; s < 4; ++s) { RDW(CADDR(c), B, s); SB(); MV(pm, (B) ^ 1, s); SB(); } } while (0)
#define RDC(c, B) do { _Pragma("unroll") for (int s = 0; s < 4; ++s) RDW(CADDR(c), B, s); SB(); } while (0)
#define MTO(pm, B) do { _Pragma("unroll") for (int s = 0; s < 4; ++s) MT(pm, B, s); SB(); } while (0)
    WBAR(15); TS(2);
    issue_k(5);
    { _Pragma("unroll") for (int s = 0; s < 4; ++s) { xf[0][s] = ldsr(lds + xa[s]); RDW(16384, 0, s); } }
    WBAR(15);
    issue_k(6); STK(1, 1, 0); WBAR(15);
    STK(2, 0, 1); WBAR(12);
    issue_c(0); issue_c(1); issue_c(2); STK(3, 1, 2);
    asm volatile("s_waitcnt vmcnt(9) lgkmcnt(0)\n\ts_barrier" : "+v"(tag) :: "memory");
    tag += 1ull;
    issue_c(3); issue_c(4); issue_c(5); STK(4, 0, 3); WBAR(9);
    issue_c(6); issue_c(7); issue_c(8); STK(5, 1, 4); WBAR(9);
    STK(6, 0, 5); MTO(6, 0);
    TS(3);
    u32x4 k0, k1;
    {
#pragma unroll
        for (int r = 0; r < 4; ++r) { acc1[r] = (padk && hh) ? 0.f : acc1[r]; acc1[12 + r] = (padk && !hh) ? 0.f : acc1[12 + r]; }
        k0 = pk4(acc1, 0); k1 = pk4(acc1, 1);
        const unsigned gaddr = WS_K + (unsigned)unit * 16384u + toff;
        __builtin_amdgcn_raw_buffer_store_b128(k0, wsr, gaddr, 0, 16);
        __builtin_amdgcn_raw_buffer_store_b128(k1, wsr, gaddr + 1024, 0, 16);
    }
    acc1 = (f32x16){};
    issue_c(9); issue_c(10); issue_c(11); issue_c(12); issue_c(13);
    WBAR(10);
    RDC(0, 1); STV(1, 0, 0); STV(2, 1, 1); STV(3, 0, 2); STV(4, 1, 3); STV(5, 0, 4);
    WBAR(5);
    if (tid == 0) __hip_atomic_store((gu64*)(ws + WS_KFLAG) + unit * 16, tag, RLX_AGENT);
    TS(5);
    STV(6, 1, 5); STV(7, 0, 6);
    TS(4);
    u32x4 v0, v1;
    {
        v0 = pk4(acc1, 0); v1 = pk4(acc1, 1);
        const bool padv = hh ? (r32 < 4) : (r32 >= 28);
        const unsigned fill = (r32 == (hh ? 0 : 28)) ? 0x3F803F80u : 0u;
        if (padv) { v0 = (u32x4){fill, fill, fill, fill}; v1 = v0; }
        const unsigned gaddr = WS_V + (unsigned)unit * 16384u + toff;
        __builtin_amdgcn_raw_buffer_store_b128(v0, wsr, gaddr, 0, 16);
        __builtin_amdgcn_raw_buffer_store_b128(v1, wsr, gaddr + 1024, 0, 16);
    }
    acc1 = (f32x16){};
    STT(8, 1, 0); WBAR(2);
    STT(9, 0, 1); STT(10, 1, 2); STT(11, 0, 3);
    unsigned long long kpoll;
    GLD8_ASYNC(kpoll, ws + WS_KFLAG, fidx * 128);
    STT(12, 1, 4); STT(13, 0, 5); MTO(6, 0);
    TS(6);
    bf16x8 Q0, Q1;
    {
        acc1[0] = (padk && hh) ? 1.f : acc1[0]; acc1[12] = (padk && !hh) ? 1.f : acc1[12];
#pragma unroll
        for (int r = 1; r < 4; ++r) { acc1[r] = (padk && hh) ? 0.f : acc1[r]; acc1[12 + r] = (padk && !hh) ? 0.f : acc1[12 + r]; }
        Q0 = BC8(pk4(acc1, 0)); Q1 = BC8(pk4(acc1, 1));
    }
    asm volatile("s_waitcnt vmcnt(0) lgkmcnt(0)\n\ts_barrier" : "+v"(kpoll) :: "memory");
    if (tid == 0) __hip_atomic_store((gu64*)(ws + WS_VFLAG) + unit * 16, tag, RLX_AGENT);
    TS(7);
    const unsigned tl = (unsigned)(tt * 8192 + hh * 2048) + lane * 16;
    auto issue_halo = [&](int isv) {
        const unsigned neg = isv ? 0u : 0xF149u;
        const u32x4 d0 = {(hh && hi == 0) ? neg : 0u, 0u, 0u, 0u}, d1 = {0u, 0u, (!hh && hi == 1) ? neg : 0u, 0u};
        const unsigned hsrc = (unsigned)unit * 16384u + toff;
        const unsigned hdst = L_KV + tt * 8192 + hh * 2048 + (isv ? 4096 : 0);
        const char* src = ws + (isv ? WS_V : WS_K);
        if (j >= 2) { glds16s_sc1(src, hsrc - 32768, lds0 + hdst); glds16s_sc1(src, hsrc - 32768 + 1024, lds0 + hdst + 1024); }
        else { *(LAS u32x4*)(ldsw + hdst + lane * 16) = d0; *(LAS u32x4*)(ldsw + hdst + 1024 + lane * 16) = d1; }
        if (j >= 1) { glds16s_sc1(src, hsrc - 16384, lds0 + 32768 + hdst); glds16s_sc1(src, hsrc - 16384 + 1024, lds0 + 32768 + hdst + 1024); }
        else { *(LAS u32x4*)(ldsw + 32768 + hdst + lane * 16) = d0; *(LAS u32x4*)(ldsw + 32768 + hdst + 1024 + lane * 16) = d1; }
    };
    if ((__ballot(kpoll == tag) & needk) != needk) wait_flags(ws + WS_KFLAG, fidx * 128, tag, needk);
    TS(8);
    issue_halo(0);
    *(LAS u32x4*)(ldsw + L_KV + 65536 + tl) = k0; *(LAS u32x4*)(ldsw + L_KV + 65536 + tl + 1024) = k1;
    *(LAS u32x4*)(ldsw + L_KV + 65536 + 4096 + tl) = v0; *(LAS u32x4*)(ldsw + L_KV + 65536 + 4096 + tl + 1024) = v1;
    const int cq = g >> 1, th = g & 1;
    const unsigned ooff = (unsigned)((lane >> 4) * 896 + min((lane & 15) ^ ((lane >> 4) << 2), 13) * 16);
    const char* wos = wo + 224 * cq;
    unsigned long long vpoll;
    WBAR(0);
    GLD8_ASYNC(vpoll, ws + WS_VFLAG, fidx * 128);
    TS(10);
    const f32x16 zero16 = {};
    const LAS char* kl = lds + L_KV + tl;
    const LAS char* vl = lds + L_KV + 4096 + tl;
    f32x16 S[9];
#pragma unroll
    for (int u = 8; u >= 0; --u) { bf16x8 a0 = ldsr(kl + u * 8192), a1 = ldsr(kl + u * 8192 + 1024); S[u] = MFMA(a0, Q0, zero16); S[u] = MFMA(a1, Q1, S[u]); }
    unsigned msk[8];
#pragma unroll
    for (int r = 0; r < 16; ++r) { const int key = (r & 3) + 8 * (r >> 2) + 4 * hi; S[8][r] = key <= r32 ? S[8][r] : S[0][r]; }
#pragma unroll
    for (int i = 0; i < 8; ++i) { const int key = ((2 * i) & 3) + 8 * ((2 * i) >> 2) + 4 * hi; msk[i] = (key <= r32 ? 0xFFFFu : 0u) | (key + 1 <= r32 ? 0xFFFF0000u : 0u); }
    float m = S[8][0];
#pragma unroll
    for (int u = 8; u >= 1; --u) m = tmax(S[u], m);
    m = xhalf_max(m) * QSCALE;
    TS(11);
    asm volatile("s_waitcnt vmcnt(0)" : "+v"(vpoll) :: "memory");
    if ((__ballot(vpoll == tag) & need) != need) wait_flags(ws + WS_VFLAG, fidx * 128, tag, need);
    TS(9);
    issue_halo(1);
    u32x4 P[9][2];
#define EXPT(u) do { _Pragma("unroll") for (int r = 0; r < 16; ++r) S[u][r] = __builtin_amdgcn_exp2f(__builtin_fmaf(S[u][r], QSCALE, -m)); \
        P[u][0] = pk4(S[u], 0); P[u][1] = pk4(S[u], 1); } while (0)
    EXPT(8); EXPT(7); EXPT(6); EXPT(5);
#pragma unroll
    for (int i = 0; i < 4; ++i) { P[0][0][i] = P[8][0][i] & ~msk[i]; P[0][1][i] = P[8][1][i] & ~msk[4 + i]; P[8][0][i] &= msk[i]; P[8][1][i] &= msk[4 + i]; }
    WBAR(0);
    TS(12);
    {
        const unsigned o1 = ooff + (unsigned)(32 * w) * 896u, l1 = lds0 + L_WO + w * 8192;
#pragma unroll
        for (int i = 0; i < 8; ++i) glds16s(wos, o1 + i * 3584, l1 + i * 1024);
    }
    f32x16 O = {};
#define PVT(u) do { bf16x8 a0 = ldsr(vl + (u) * 8192), a1 = ldsr(vl + (u) * 8192 + 1024); O = MFMA(a0, BC8(P[u][0]), O); O = MFMA(a1, BC8(P[u][1]), O); } while (0)
    PVT(8); EXPT(4); PVT(7); EXPT(3); PVT(6); EXPT(2); PVT(5); EXPT(1); PVT(4); PVT(3); PVT(2); PVT(1); PVT(0);
    TS(13);
    {
        const float lsrc = hh ? O[0] : O[12];
        auto rr = __builtin_amdgcn_permlane32_swap(__float_as_uint(lsrc), __float_as_uint(lsrc), false, false);
        const float l = hh ? __uint_as_float(rr[0]) : __uint_as_float(rr[1]);
        const float inv = __builtin_amdgcn_rcpf(l);
#pragma unroll
        for (int r = 0; r < 16; ++r) O[r] *= inv;
#pragma unroll
        for (int r = 0; r < 4; ++r) { O[r] = (padk && hh) ? 0.f : O[r]; O[12 + r] = (padk && !hh) ? 0.f : O[12 + r]; }
        u32x4 o0 = pk4(O, 0), o1 = pk4(O, 1);
#pragma unroll
        for (int i = 0; i < 4; ++i) { o0[i] = min(o0[i], 0xFFFFFFFEu); o1[i] = min(o1[i], 0xFFFFFFFEu); }
        WAITV(0);
        __builtin_amdgcn_raw_buffer_store_b128(o0, wsr, oaddr, 0, 16);
        __builtin_amdgcn_raw_buffer_store_b128(o1, wsr, oaddr + 1024, 0, 16);
    }
    LBAR();
    TS(14);
    {
        const unsigned o2 = ooff + (unsigned)(256 + 24 * w) * 896u, l2 = lds0 + (24 * w) * 256;
#pragma unroll
        for (int i = 0; i < 6; ++i) glds16s(wos, o2 + i * 3584, l2 + i * 1024);
    }
    u32x4 of[8];
    const unsigned ga = WS_O + (unsigned)(((b * 64 + 4 * j + 2 * th) * 16 + 2 * w) * 2048) + lane * 16;
    const int t2 = w & 1, nt = w >> 1;
    const int no = nt == 3 ? 80 : 32 * nt;
    const unsigned aa = (unsigned)((4 * hi + q4) * 256 + (((no >> 3) + 2 * blk + (p4 >> 1)) ^ (q4 << 2)) * 16 + 8 * (p4 & 1));
    bf16x8 af[32];
    const LAS char* ab1 = lds + (aa + L_WO);
    const LAS char* ab2 = lds + aa;
    asm volatile("" : "+v"(ab1));
#define APTR(r) ((r) < 256 ? ab1 + (r) * 256 : ab2 + ((r) - 256) * 256)
#define AFRAG(h, s) af[2 * (h) + (s)] = ldstr2(APTR(28 * (h) - 4 * ((h) & 1) + 16 * (s)), 8 * 256)
#pragma unroll
    for (int h = 0; h < 9; ++h) { AFRAG(h, 0); AFRAG(h, 1); }
    const s16x4 a9lo = ldstr(APTR(248));
    LBAR();
    TS(15);
    if (bid == 0 && w == 7) {
        for (unsigned spins = 0; spins <= SPIN_CAP; ++spins) {
            unsigned long long f0, f1, f2, f3;
            asm volatile("global_load_dwordx2 %0, %4, %5 sc1\n\tglobal_load_dwordx2 %1, %4, %5 offset:128 sc1\n\t"
                         "global_load_dwordx2 %2, %4, %5 offset:256 sc1\n\tglobal_load_dwordx2 %3, %4, %5 offset:384 sc1\n\ts_waitcnt vmcnt(0)"
                         : "=&v"(f0), "=&v"(f1), "=&v"(f2), "=&v"(f3) : "v"(lane * 512), "s"(ws + WS_KFLAG) : "memory");
            if (__all(f0 == tag && f1 == tag && f2 == tag && f3 == tag)) break;
            __builtin_amdgcn_s_sleep(1);
        }
        if (lane == 0) __hip_atomic_store((gu64*)(ws + WS_GEN), tag, RLX_AGENT);
    }
    for (unsigned spins = 0;; ++spins) {
        gld_o(of, ws, ga, ga + 32768);
        unsigned mx = 0;
#pragma unroll
        for (int i = 0; i < 8; ++i) mx = max(max(mx, max(of[i][0], of[i][1])), max(of[i][2], of[i][3]));
        if (__all(mx != 0xFFFFFFFFu) || spins > SPIN_CAP) break;
    }
#pragma unroll
    for (int i = 0; i < 4; ++i) {
        *(LAS u32x4*)(ldsw + L_WO + (2 * w) * 2048 + i * 1024 + lane * 16) = of[i];
        *(LAS u32x4*)(ldsw + L_WO + 32768 + (2 * w) * 2048 + i * 1024 + lane * 16) = of[4 + i];
    }
    TS(16);
    LBAR();
    TS(17);
    f32x16 acc = {};
    const LAS char* ol = lds + L_WO + t2 * 32768 + lane * 16;
    {
        bf16x8 bq[18];
#pragma unroll
        for (int i = 0; i < 18; ++i) bq[i] = ldsr(ol + i * 1024);
        { const s16x4 a9hi = ldstr(APTR(256)); af[18] = __builtin_shufflevector(a9lo, a9hi, 0, 1, 2, 3, 4, 5, 6, 7); }
        AFRAG(9, 1);
#pragma unroll
        for (int h = 10; h < 16; ++h) { AFRAG(h, 0); AFRAG(h, 1); }
#pragma unroll
        for (int i = 0; i < 18; ++i) acc = MFMA(af[i], bq[i], acc);
    }
    {
        bf16x8 bq[14];
#pragma unroll
        for (int i = 0; i < 14; ++i) bq[i] = ldsr(ol + (18 + i) * 1024);
#pragma unroll
        for (int i = 0; i < 14; ++i) acc = MFMA(af[18 + i], bq[i], acc);
    }
    TS(18);
    {
        u16* orow = out + (size_t)(128 * j + 64 * th + 32 * t2 + r32) * 896 + 448 * b + 112 * cq + no + (hi ? 8 : 0);
#pragma unroll
        for (int pr = 0; pr < 2; ++pr) {
            unsigned ax = pk(acc[8 * pr], acc[8 * pr + 1]), ay = pk(acc[8 * pr + 2], acc[8 * pr + 3]);
            unsigned bx = pk(acc[8 * pr + 4], acc[8 * pr + 5]), by = pk(acc[8 * pr + 6], acc[8 * pr + 7]);
            auto rx = __builtin_amdgcn_permlane32_swap(ax, bx, false, false);
            auto ry = __builtin_amdgcn_permlane32_swap(ay, by, false, false);
            if (!(nt == 3 && pr == 0)) *(u32x4*)(orow + 16 * pr) = (u32x4){rx[0], ry[0], rx[1], ry[1]};
        }
    }
}

extern "C" void kernel_launch(void* const* d_in, const int* in_sizes, int n_in, void* d_out, int out_size, void* d_ws, size_t ws_size, hipStream_t stream) {
    (void)in_sizes; (void)n_in; (void)out_size; (void)ws_size;
    (void)hipFuncSetAttribute((const void*)fused_kernel, hipFuncAttributeMaxDynamicSharedMemorySize, 160 * 1024);
    fused_kernel<<<256, 512, 160 * 1024, stream>>>((const u16*)d_in[0], (const u16*)d_in[1], (const u16*)d_in[2], (const u16*)d_in[3], (const u16*)d_in[4], (const u16*)d_in[5], (u16*)d_out, (char*)d_ws);
}
```

```cpp
#include <hip/hip_runtime.h>
#include <hip/hip_bf16.h>
#include <stdint.h>

typedef unsigned short u16;
typedef short bf16x8 __attribute__((ext_vector_type(8)));
typedef short s16x4 __attribute__((ext_vector_type(4)));
typedef short v4i16_t __attribute__((ext_vector_type(4)));
typedef float f32x16 __attribute__((ext_vector_type(16)));
typedef float f32x2 __attribute__((ext_vector_type(2)));
typedef __bf16 bf16x2_t __attribute__((ext_vector_type(2)));
typedef unsigned u32x4 __attribute__((ext_vector_type(4)));
#define LAS __attribute__((address_space(3)))
#define GAS __attribute__((address_space(1)))
typedef GAS unsigned long long gu64;
#define RLX_AGENT __ATOMIC_RELAXED, __HIP_MEMORY_SCOPE_AGENT
#define DI __device__ __forceinline__
#define MFMA(a, b, c) __builtin_amdgcn_mfma_f32_32x32x16_bf16((a), (b), (c), 0, 0, 0)
#define LBAR() asm volatile("s_waitcnt lgkmcnt(0)\n\ts_barrier" ::: "memory")
#define WAITV(N) asm volatile("s_waitcnt vmcnt(" #N ")" ::: "memory")
#define WBAR(N) asm volatile("s_waitcnt vmcnt(" #N ") lgkmcnt(0)\n\ts_barrier" ::: "memory")

constexpr unsigned WS_GEN = 0, WS_KFLAG = 65536, WS_VFLAG = 131072, WS_K = 1u << 20, WS_V = 6u << 20, WS_O = 16u << 20, WS_BYTES = 24u << 20;
constexpr float QSCALE = 0.1889822365046136f * 1.4426950408889634f;
constexpr unsigned SPIN_CAP = 1u << 18;
constexpr unsigned RSLOT = 24576;
constexpr unsigned L_KV = 0, L_WO = 98304;

DI void glds16s(const void* sbase, unsigned voff, unsigned lds_dst) {
    asm volatile("s_mov_b32 m0, %2\n\ts_nop 0\n\tglobal_load_lds_dwordx4 %0, %1" :: "v"(voff), "s"(sbase), "s"(lds_dst) : "memory");
}
DI void glds16s_sc1(const void* sbase, unsigned voff, unsigned lds_dst) {
    asm volatile("s_mov_b32 m0, %2\n\ts_nop 0\n\tglobal_load_lds_dwordx4 %0, %1 sc1" :: "v"(voff), "s"(sbase), "s"(lds_dst) : "memory");
}
DI unsigned pk(float a, float b) { f32x2 v = {a, b}; bf16x2_t r = __builtin_convertvector(v, bf16x2_t); return __builtin_bit_cast(unsigned, r); }
DI bf16x8 ldsr(const LAS char* p) { return *(const LAS bf16x8*)p; }
DI s16x4 ldstr(const LAS char* p) { return __builtin_bit_cast(s16x4, __builtin_amdgcn_ds_read_tr16_b64_v4i16((LAS v4i16_t*)p)); }
DI bf16x8 ldstr2(const LAS char* p, int off2) { s16x4 a = ldstr(p), b = ldstr(p + off2); return __builtin_shufflevector(a, b, 0, 1, 2, 3, 4, 5, 6, 7); }
DI float max3(float a, float b, float c) { return __builtin_fmaxf(__builtin_fmaxf(a, b), c); }
DI float tmax(const f32x16& p, float m) {
    float a = max3(m, p[0], p[1]), b = max3(p[2], p[3], p[4]);
    a = max3(a, p[5], p[6]); b = max3(b, p[7], p[8]); a = max3(a, p[9], p[10]); b = max3(b, p[11], p[12]); a = max3(a, p[13], p[14]);
    return max3(a, b, p[15]);
}
DI float xhalf_max(float m) { auto rr = __builtin_amdgcn_permlane32_swap(__float_as_uint(m), __float_as_uint(m), false, false); return __builtin_fmaxf(__uint_as_float(rr[0]), __uint_as_float(rr[1])); }
DI u32x4 pk4(const f32x16& a, int s) { return (u32x4){pk(a[8 * s], a[8 * s + 1]), pk(a[8 * s + 2], a[8 * s + 3]), pk(a[8 * s + 4], a[8 * s + 5]), pk(a[8 * s + 6], a[8 * s + 7])}; }
#define BC8(x) __builtin_bit_cast(bf16x8, x)

DI void gld_o(u32x4 (&r)[8], const char* sbase, unsigned va, unsigned vb) {
    asm volatile("global_load_dwordx4 %0, %8, %10 sc1\n\tglobal_load_dwordx4 %1, %8, %10 offset:1024 sc1\n\t"
                 "global_load_dwordx4 %2, %8, %10 offset:2048 sc1\n\tglobal_load_dwordx4 %3, %8, %10 offset:3072 sc1\n\t"
                 "global_load_dwordx4 %4, %9, %10 sc1\n\tglobal_load_dwordx4 %5, %9, %10 offset:1024 sc1\n\t"
                 "global_load_dwordx4 %6, %9, %10 offset:2048 sc1\n\tglobal_load_dwordx4 %7, %9, %10 offset:3072 sc1\n\t"
                 "s_waitcnt vmcnt(0)"
                 : "=&v"(r[0]), "=&v"(r[1]), "=&v"(r[2]), "=&v"(r[3]), "=&v"(r[4]), "=&v"(r[5]), "=&v"(r[6]), "=&v"(r[7])
                 : "v"(va), "v"(vb), "s"(sbase) : "memory");
}
DI unsigned long long gld8_wait(const char* sbase, unsigned voff) {
    unsigned long long r;
    asm volatile("global_load_dwordx2 %0, %1, %2 sc1\n\ts_waitcnt vmcnt(0)" : "=&v"(r) : "v"(voff), "s"(sbase) : "memory");
    return r;
}
#define GLD8_ASYNC(dst, sbase, voff) asm volatile("global_load_dwordx2 %0, %1, %2 sc1" : "=v"(dst) : "v"(voff), "s"(sbase) : "memory")
DI void wait_flags(const char* sbase, unsigned voff, unsigned long long tag, unsigned long long need) {
    for (unsigned spins = 0; spins <= SPIN_CAP; ++spins) {
        const unsigned long long v = gld8_wait(sbase, voff);
        if ((__ballot(v == tag) & need) == need) break;
        __builtin_amdgcn_s_sleep(1);
    }
}

__global__ void __launch_bounds__(512, 2)
fused_kernel(const u16* __restrict__ xA, const u16* __restrict__ wA, const u16* __restrict__ oA,
             const u16* __restrict__ xB, const u16* __restrict__ wB, const u16* __restrict__ oB,
             u16* __restrict__ out, char* ws)
{
    extern __shared__ __attribute__((aligned(16))) char lds_[];
    const LAS char* lds = (const LAS char*)lds_;
    LAS char* ldsw = (LAS char*)lds_;
    const unsigned lds0 = (unsigned)(uintptr_t)lds_;
    const int tid = threadIdx.x, lane = tid & 63;
    const int w = __builtin_amdgcn_readfirstlane(tid >> 6);
    const int bid = blockIdx.x, xcd = bid & 7, idx = bid >> 3;
    const int b = xcd >> 2, g = idx & 7, j = (xcd & 3) * 4 + (idx >> 3);
    const int hh = w >> 2, tt = w & 3;
    const char* x = (const char*)(b ? xB : xA) + (size_t)j * 128 * 896;
    const char* wq = (const char*)(b ? wB : wA) + 112 * g;
    const int rot = (g + j) % 7;
    const unsigned xo0 = (unsigned)((16 * w + (lane >> 3)) * 896 + (((lane & 7) ^ (lane >> 4)) * 16));
    const unsigned xo1 = (unsigned)((16 * w + 8 + (lane >> 3)) * 896 + (((lane & 7) ^ (4 + (lane >> 4))) * 16));
    const unsigned wo1 = (unsigned)((8 * w + (lane >> 3)) * 2688 + ((((lane >> 2) & 1) ^ ((lane >> 4) & 1)) * 48) + (lane & 3) * 16);
    unsigned kcs[7];
#pragma unroll
    for (int p = 0; p < 7; ++p) { const int k = p + rot; kcs[p] = (unsigned)(k >= 7 ? k - 7 : k); }
    const unsigned ldx = lds0 + w * 2048, ldw = lds0 + w * 1024;
    auto issue_w = [&](int which, int p, unsigned dst) { glds16s(wq, wo1 + kcs[p] * (64u * 2688u) + which * 896, ldw + dst); };
    auto issue_k = [&](int p) {
        const unsigned slot = (p % 6) * RSLOT;
        glds16s(x, xo0 + kcs[p] * 128u, ldx + slot); glds16s(x, xo1 + kcs[p] * 128u, ldx + slot + 1024);
        issue_w(1, p, slot + 16384);
    };
#define CADDR(c) (((7 + (c) / 3) % 6) * RSLOT + ((c) % 3) * 8192)
    auto issue_c = [&](int c) { issue_w(c < 7 ? 2 : 0, c < 7 ? c : c - 7, CADDR(c)); };
    issue_k(0); issue_k(1); issue_k(2); issue_k(3); issue_k(4);
    const char* wo = (const char*)(b ? oB : oA);
    const int r32 = lane & 31, hi = lane >> 5;
    const __amdgpu_buffer_rsrc_t wsr = __builtin_amdgcn_make_buffer_rsrc(ws, 0, WS_BYTES, 0x00020000);
    const int unit = (b * 8 + g) * 16 + j;
    const int q4 = (lane & 15) >> 2, p4 = lane & 3, blk = (lane >> 4) & 1;
    unsigned long long tag;
    GLD8_ASYNC(tag, ws + WS_GEN, 0u);
    const unsigned oaddr = WS_O + (unsigned)(((b * 64 + 4 * j + tt) * 16 + 2 * g + hh) * 2048) + lane * 16;
    {
        const u32x4 poison = {0xFFFFFFFFu, 0xFFFFFFFFu, 0xFFFFFFFFu, 0xFFFFFFFFu};
        __builtin_amdgcn_raw_buffer_store_b128(poison, wsr, oaddr, 0, 16);
        __builtin_amdgcn_raw_buffer_store_b128(poison, wsr, oaddr + 1024, 0, 16);
    }
    int fidx = unit;
    if (lane == 0) fidx = j >= 1 ? unit - 1 : unit;
    else if (lane == 1) fidx = j >= 2 ? unit - 2 : unit;
    else if (lane < 10) fidx = (b * 8 + (lane - 2)) * 16 + j;
    const unsigned long long need = (j >= 2 ? 3ull : (j >= 1 ? 1ull : 0ull)), needk = need | 0x3FCull;

    unsigned xa[4];
#pragma unroll
    for (int s = 0; s < 4; ++s) xa[s] = (unsigned)((32 * tt + r32) * 128 + (((hi ^ ((r32 >> 1) & 7)) * 16) ^ (s * 32)));
    const unsigned wa = (unsigned)((8 * hi + q4) * 128 + ((hh ^ (q4 >> 1)) * 64) + 32 * blk + 8 * p4);
    const bool padk = hh ? (hi == 0) : (hi == 1);
    const unsigned toff = (unsigned)(hh * 8192 + tt * 2048) + lane * 16;

    bf16x8 xf[7][4], fr[2][4];
    f32x16 acc1 = {};
#define SB() __builtin_amdgcn_sched_barrier(0)
#define KB(p) (((p) % 6) * RSLOT)
#define RDW(base, B, s) fr[B][s] = ldstr2(lds + (base) + wa + (s) * 2048, 512)
#define MT(p, B, s) acc1 = MFMA(fr[B][s], xf[p][s], acc1)
#define MV(p, B, s) acc1 = MFMA(xf[p][s], fr[B][s], acc1)
#define STK(p, B, pm) do { _Pragma("unroll") for (int s = 0; s < 4; ++s) { xf[p][s] = ldsr(lds + KB(p) + xa[s]); RDW(KB(p) + 16384, B, s); SB(); MT(pm, (B) ^ 1, s); SB(); } } while (0)
#define STT(c, B, pm) do { _Pragma("unroll") for (int s = 0; s < 4; ++s) { RDW(CADDR(c), B, s); SB(); MT(pm, (B) ^ 1, s); SB(); } } while (0)
#define STV(c, B, pm) do { _Pragma("unroll") for (int s = 0; s < 4; ++s) { RDW(CADDR(c), B, s); SB(); MV(pm, (B) ^ 1, s); SB(); } } while (0)
#define RDC(c, B) do { _Pragma("unroll") for (int s = 0; s < 4; ++s) RDW(CADDR(c), B, s); SB(); } while (0)
#define MTO(pm, B) do { _Pragma("unroll") for (int s = 0; s < 4; ++s) MT(pm, B, s); SB(); } while (0)
    WBAR(15);
    issue_k(5);
    { _Pragma("unroll") for (int s = 0; s < 4; ++s) { xf[0][s] = ldsr(lds + xa[s]); RDW(16384, 0, s); } }
    WBAR(15);
    issue_k(6); STK(1, 1, 0); WBAR(15);
    STK(2, 0, 1); WBAR(12);
    issue_c(0); issue_c(1); issue_c(2); STK(3, 1, 2);
    asm volatile("s_waitcnt vmcnt(9) lgkmcnt(0)\n\ts_barrier" : "+v"(tag) :: "memory");
    tag += 1ull;
    issue_c(3); issue_c(4); issue_c(5); STK(4, 0, 3); WBAR(9);
    issue_c(6); issue_c(7); issue_c(8); STK(5, 1, 4); WBAR(9);
    STK(6, 0, 5); MTO(6, 0);
    u32x4 k0, k1;
    {
#pragma unroll
        for (int r = 0; r < 4; ++r) { acc1[r] = (padk && hh) ? 0.f : acc1[r]; acc1[12 + r] = (padk && !hh) ? 0.f : acc1[12 + r]; }
        k0 = pk4(acc1, 0); k1 = pk4(acc1, 1);
        const unsigned gaddr = WS_K + (unsigned)unit * 16384u + toff;
        __builtin_amdgcn_raw_buffer_store_b128(k0, wsr, gaddr, 0, 16);
        __builtin_amdgcn_raw_buffer_store_b128(k1, wsr, gaddr + 1024, 0, 16);
    }
    acc1 = (f32x16){};
    issue_c(9); issue_c(10); issue_c(11); issue_c(12); issue_c(13);
    WBAR(10);
    RDC(0, 1); STV(1, 0, 0); STV(2, 1, 1); STV(3, 0, 2); STV(4, 1, 3); STV(5, 0, 4);
    WBAR(5);
    if (tid == 0) __hip_atomic_store((gu64*)(ws + WS_KFLAG) + unit * 16, tag, RLX_AGENT);
    STV(6, 1, 5); STV(7, 0, 6);
    u32x4 v0, v1;
    {
        v0 = pk4(acc1, 0); v1 = pk4(acc1, 1);
        const bool padv = hh ? (r32 < 4) : (r32 >= 28);
        const unsigned fill = (r32 == (hh ? 0 : 28)) ? 0x3F803F80u : 0u;
        if (padv) { v0 = (u32x4){fill, fill, fill, fill}; v1 = v0; }
        const unsigned gaddr = WS_V + (unsigned)unit * 16384u + toff;
        __builtin_amdgcn_raw_buffer_store_b128(v0, wsr, gaddr, 0, 16);
        __builtin_amdgcn_raw_buffer_store_b128(v1, wsr, gaddr + 1024, 0, 16);
    }
    acc1 = (f32x16){};
    STT(8, 1, 0); WBAR(2);
    STT(9, 0, 1); STT(10, 1, 2); STT(11, 0, 3);
    unsigned long long kpoll;
    GLD8_ASYNC(kpoll, ws + WS_KFLAG, fidx * 128);
    STT(12, 1, 4); STT(13, 0, 5); MTO(6, 0);
    bf16x8 Q0, Q1;
    {
        acc1[0] = (padk && hh) ? 1.f : acc1[0]; acc1[12] = (padk && !hh) ? 1.f : acc1[12];
#pragma unroll
        for (int r = 1; r < 4; ++r) { acc1[r] = (padk && hh) ? 0.f : acc1[r]; acc1[12 + r] = (padk && !hh) ? 0.f : acc1[12 + r]; }
        Q0 = BC8(pk4(acc1, 0)); Q1 = BC8(pk4(acc1, 1));
    }
    asm volatile("s_waitcnt vmcnt(0) lgkmcnt(0)\n\ts_barrier" : "+v"(kpoll) :: "memory");
    if (tid == 0) __hip_atomic_store((gu64*)(ws + WS_VFLAG) + unit * 16, tag, RLX_AGENT);
    const unsigned tl = (unsigned)(tt * 8192 + hh * 2048) + lane * 16;
    auto issue_halo = [&](int isv) {
        const unsigned neg = isv ? 0u : 0xF149u;
        const u32x4 d0 = {(hh && hi == 0) ? neg : 0u, 0u, 0u, 0u}, d1 = {0u, 0u, (!hh && hi == 1) ? neg : 0u, 0u};
        const unsigned hsrc = (unsigned)unit * 16384u + toff;
        const unsigned hdst = L_KV + tt * 8192 + hh * 2048 + (isv ? 4096 : 0);
        const char* src = ws + (isv ? WS_V : WS_K);
        if (j >= 2) { glds16s_sc1(src, hsrc - 32768, lds0 + hdst); glds16s_sc1(src, hsrc - 32768 + 1024, lds0 + hdst + 1024); }
        else { *(LAS u32x4*)(ldsw + hdst + lane * 16) = d0; *(LAS u32x4*)(ldsw + hdst + 1024 + lane * 16) = d1; }
        if (j >= 1) { glds16s_sc1(src, hsrc - 16384, lds0 + 32768 + hdst); glds16s_sc1(src, hsrc - 16384 + 1024, lds0 + 32768 + hdst + 1024); }
        else { *(LAS u32x4*)(ldsw + 32768 + hdst + lane * 16) = d0; *(LAS u32x4*)(ldsw + 32768 + hdst + 1024 + lane * 16) = d1; }
    };
    if ((__ballot(kpoll == tag) & needk) != needk) wait_flags(ws + WS_KFLAG, fidx * 128, tag, needk);
    issue_halo(0);
    *(LAS u32x4*)(ldsw + L_KV + 65536 + tl) = k0; *(LAS u32x4*)(ldsw + L_KV + 65536 + tl + 1024) = k1;
    *(LAS u32x4*)(ldsw + L_KV + 65536 + 4096 + tl) = v0; *(LAS u32x4*)(ldsw + L_KV + 65536 + 4096 + tl + 1024) = v1;
    const int cq = g >> 1, th = g & 1;
    const unsigned ooff = (unsigned)((lane >> 4) * 896 + min((lane & 15) ^ ((lane >> 4) << 2), 13) * 16);
    const char* wos = wo + 224 * cq;
    unsigned long long vpoll;
    WBAR(0);
    GLD8_ASYNC(vpoll, ws + WS_VFLAG, fidx * 128);
    const f32x16 zero16 = {};
    const LAS char* kl = lds + L_KV + tl;
    const LAS char* vl = lds + L_KV + 4096 + tl;
    f32x16 S[9];
#pragma unroll
    for (int u = 8; u >= 0; --u) { bf16x8 a0 = ldsr(kl + u * 8192), a1 = ldsr(kl + u * 8192 + 1024); S[u] = MFMA(a0, Q0, zero16); S[u] = MFMA(a1, Q1, S[u]); }
    unsigned msk[8];
#pragma unroll
    for (int r = 0; r < 16; ++r) { const int key = (r & 3) + 8 * (r >> 2) + 4 * hi; S[8][r] = key <= r32 ? S[8][r] : S[0][r]; }
#pragma unroll
    for (int i = 0; i < 8; ++i) { const int key = ((2 * i) & 3) + 8 * ((2 * i) >> 2) + 4 * hi; msk[i] = (key <= r32 ? 0xFFFFu : 0u) | (key + 1 <= r32 ? 0xFFFF0000u : 0u); }
    float m = S[8][0];
#pragma unroll
    for (int u = 8; u >= 1; --u) m = tmax(S[u], m);
    m = xhalf_max(m) * QSCALE;
    asm volatile("s_waitcnt vmcnt(0)" : "+v"(vpoll) :: "memory");
    if ((__ballot(vpoll == tag) & need) != need) wait_flags(ws + WS_VFLAG, fidx * 128, tag, need);
    issue_halo(1);
    u32x4 P[9][2];
#define EXPT(u) do { _Pragma("unroll") for (int r = 0; r < 16; ++r) S[u][r] = __builtin_amdgcn_exp2f(__builtin_fmaf(S[u][r], QSCALE, -m)); \
        P[u][0] = pk4(S[u], 0); P[u][1] = pk4(S[u], 1); } while (0)
    EXPT(8); EXPT(7); EXPT(6); EXPT(5);
#pragma unroll
    for (int i = 0; i < 4; ++i) { P[0][0][i] = P[8][0][i] & ~msk[i]; P[0][1][i] = P[8][1][i] & ~msk[4 + i]; P[8][0][i] &= msk[i]; P[8][1][i] &= msk[4 + i]; }
    WBAR(0);
    {
        const unsigned o1 = ooff + (unsigned)(32 * w) * 896u, l1 = lds0 + L_WO + w * 8192;
#pragma unroll
        for (int i = 0; i < 8; ++i) glds16s(wos, o1 + i * 3584, l1 + i * 1024);
    }
    f32x16 O = {};
#define PVT(u) do { bf16x8 a0 = ldsr(vl + (u) * 8192), a1 = ldsr(vl + (u) * 8192 + 1024); O = MFMA(a0, BC8(P[u][0]), O); O = MFMA(a1, BC8(P[u][1]), O); } while (0)
    PVT(8); EXPT(4); PVT(7); EXPT(3); PVT(6); EXPT(2); PVT(5); EXPT(1); PVT(4); PVT(3); PVT(2); PVT(1); PVT(0);
    {
        const float lsrc = hh ? O[0] : O[12];
        auto rr = __builtin_amdgcn_permlane32_swap(__float_as_uint(lsrc), __float_as_uint(lsrc), false, false);
        const float l = hh ? __uint_as_float(rr[0]) : __uint_as_float(rr[1]);
        const float inv = __builtin_amdgcn_rcpf(l);
#pragma unroll
        for (int r = 0; r < 16; ++r) O[r] *= inv;
#pragma unroll
        for (int r = 0; r < 4; ++r) { O[r] = (padk && hh) ? 0.f : O[r]; O[12 + r] = (padk && !hh) ? 0.f : O[12 + r]; }
        u32x4 o0 = pk4(O, 0), o1 = pk4(O, 1);
#pragma unroll
        for (int i = 0; i < 4; ++i) { o0[i] = min(o0[i], 0xFFFFFFFEu); o1[i] = min(o1[i], 0xFFFFFFFEu); }
        WAITV(0);
        __builtin_amdgcn_raw_buffer_store_b128(o0, wsr, oaddr, 0, 16);
        __builtin_amdgcn_raw_buffer_store_b128(o1, wsr, oaddr + 1024, 0, 16);
    }
    LBAR();
    {
        const unsigned o2 = ooff + (unsigned)(256 + 24 * w) * 896u, l2 = lds0 + (24 * w) * 256;
#pragma unroll
        for (int i = 0; i < 6; ++i) glds16s(wos, o2 + i * 3584, l2 + i * 1024);
    }
    u32x4 of[8];
    const unsigned ga = WS_O + (unsigned)(((b * 64 + 4 * j + 2 * th) * 16 + 2 * w) * 2048) + lane * 16;
    const int t2 = w & 1, nt = w >> 1;
    const int no = nt == 3 ? 80 : 32 * nt;
    const unsigned aa = (unsigned)((4 * hi + q4) * 256 + (((no >> 3) + 2 * blk + (p4 >> 1)) ^ (q4 << 2)) * 16 + 8 * (p4 & 1));
    bf16x8 af[32];
    const LAS char* ab1 = lds + (aa + L_WO);
    const LAS char* ab2 = lds + aa;
    asm volatile("" : "+v"(ab1));
#define APTR(r) ((r) < 256 ? ab1 + (r) * 256 : ab2 + ((r) - 256) * 256)
#define AFRAG(h, s) af[2 * (h) + (s)] = ldstr2(APTR(28 * (h) - 4 * ((h) & 1) + 16 * (s)), 8 * 256)
#pragma unroll
    for (int h = 0; h < 9; ++h) { AFRAG(h, 0); AFRAG(h, 1); }
    const s16x4 a9lo = ldstr(APTR(248));
    LBAR();
    if (bid == 0 && w == 7) {
        for (unsigned spins = 0; spins <= SPIN_CAP; ++spins) {
            unsigned long long f0, f1, f2, f3;
            asm volatile("global_load_dwordx2 %0, %4, %5 sc1\n\tglobal_load_dwordx2 %1, %4, %5 offset:128 sc1\n\t"
                         "global_load_dwordx2 %2, %4, %5 offset:256 sc1\n\tglobal_load_dwordx2 %3, %4, %5 offset:384 sc1\n\ts_waitcnt vmcnt(0)"
                         : "=&v"(f0), "=&v"(f1), "=&v"(f2), "=&v"(f3) : "v"(lane * 512), "s"(ws + WS_KFLAG) : "memory");
            if (__all(f0 == tag && f1 == tag && f2 == tag && f3 == tag)) break;
            __builtin_amdgcn_s_sleep(1);
        }
        if (lane == 0) __hip_atomic_store((gu64*)(ws + WS_GEN), tag, RLX_AGENT);
    }
    for (unsigned spins = 0;; ++spins) {
        gld_o(of, ws, ga, ga + 32768);
        unsigned mx = 0;
#pragma unroll
        for (int i = 0; i < 8; ++i) mx = max(max(mx, max(of[i][0], of[i][1])), max(of[i][2], of[i][3]));
        if (__all(mx != 0xFFFFFFFFu) || spins > SPIN_CAP) break;
    }
#pragma unroll
    for (int i = 0; i < 4; ++i) {
        *(LAS u32x4*)(ldsw + L_WO + (2 * w) * 2048 + i * 1024 + lane * 16) = of[i];
        *(LAS u32x4*)(ldsw + L_WO + 32768 + (2 * w) * 2048 + i * 1024 + lane * 16) = of[4 + i];
    }
    LBAR();
    f32x16 acc = {};
    const LAS char* ol = lds + L_WO + t2 * 32768 + lane * 16;
    {
        bf16x8 bq[18];
#pragma unroll
        for (int i = 0; i < 18; ++i) bq[i] = ldsr(ol + i * 1024);
        { const s16x4 a9hi = ldstr(APTR(256)); af[18] = __builtin_shufflevector(a9lo, a9hi, 0, 1, 2, 3, 4, 5, 6, 7); }
        AFRAG(9, 1);
#pragma unroll
        for (int h = 10; h < 16; ++h) { AFRAG(h, 0); AFRAG(h, 1); }
#pragma unroll
        for (int i = 0; i < 18; ++i) acc = MFMA(af[i], bq[i], acc);
    }
    {
        bf16x8 bq[14];
#pragma unroll
        for (int i = 0; i < 14; ++i) bq[i] = ldsr(ol + (18 + i) * 1024);
#pragma unroll
        for (int i = 0; i < 14; ++i) acc = MFMA(af[18 + i], bq[i], acc);
    }
    {
        u16* orow = out + (size_t)(128 * j + 64 * th + 32 * t2 + r32) * 896 + 448 * b + 112 * cq + no + (hi ? 8 : 0);
#pragma unroll
        for (int pr = 0; pr < 2; ++pr) {
            unsigned ax = pk(acc[8 * pr], acc[8 * pr + 1]), ay = pk(acc[8 * pr + 2], acc[8 * pr + 3]);
            unsigned bx = pk(acc[8 * pr + 4], acc[8 * pr + 5]), by = pk(acc[8 * pr + 6], acc[8 * pr + 7]);
            auto rx = __builtin_amdgcn_permlane32_swap(ax, bx, false, false);
            auto ry = __builtin_amdgcn_permlane32_swap(ay, by, false, false);
            if (!(nt == 3 && pr == 0)) *(u32x4*)(orow + 16 * pr) = (u32x4){rx[0], ry[0], rx[1], ry[1]};
        }
    }
}

extern "C" void kernel_launch(void* const* d_in, const int* in_sizes, int n_in, void* d_out, int out_size, void* d_ws, size_t ws_size, hipStream_t stream) {
    (void)in_sizes; (void)n_in; (void)out_size; (void)ws_size;
    (void)hipFuncSetAttribute((const void*)fused_kernel, hipFuncAttributeMaxDynamicSharedMemorySize, 160 * 1024);
    fused_kernel<<<256, 512, 160 * 1024, stream>>>((const u16*)d_in[0], (const u16*)d_in[1], (const u16*)d_in[2], (const u16*)d_in[3], (const u16*)d_in[4], (const u16*)d_in[5], (u16*)d_out, (char*)d_ws);
}
```
